```python
import torch

def _softmax(x, axis=-1):
    return torch.softmax(x, dim=axis)
def _gelu(x):
    return torch.nn.functional.gelu(x, approximate="tanh")
def _silu(x):
    return torch.nn.functional.silu(x)
def _sigmoid(x):
    return torch.sigmoid(x)
def _layernorm(x, axis=-1, eps=1e-5):
    m = x.mean(dim=axis, keepdim=True)
    v = x.var(dim=axis, keepdim=True, unbiased=False)
    return (x - m) / torch.sqrt(v + eps)
def _scatter_add(tgt, idx, src, axis=0):
    return tgt.clone().index_add_(axis, idx, src)
def _segment_sum(src, seg, n):
    out = torch.zeros((n,) + src.shape[1:], dtype=src.dtype)
    return out.index_add_(0, seg, src)
def _pad(x, pw):
    flat = tuple(v for pair in reversed(pw) for v in pair)
    return torch.nn.functional.pad(x, flat)

kernel_name = 'standalone__deep_cross_pnn__recsys'

def setup_inputs():

    _w = (lambda a: (lambda t: t.to(torch.bfloat16) if t.is_floating_point() else t)(torch.as_tensor(a)))
    B, F, D, V = 16384, 100, 64, min(100000, 100000)
    H = 4 * D
    return {
        "idx": _w((lambda lo, hi, *s: torch.randint(lo, hi, s))(0, V, B, F).to(torch.int64)),
        "table": _w((lambda *s: torch.randn(*s, dtype=torch.bfloat16) * 1.6)(V, D) * (D ** -0.5)),
        "w_p": _w((lambda *s: torch.randn(*s, dtype=torch.bfloat16) * 0.05)(F * F) * ((F * F) ** -0.5)),
        "w1": _w((lambda *s: torch.randn(*s, dtype=torch.bfloat16) * 1.6)(F * D, H) * ((F * D) ** -0.5)),
        "b1": _w((lambda *s: torch.randn(*s, dtype=torch.bfloat16) * 12.8)(H) * 0.01),
        "w2": _w((lambda *s: torch.randn(*s, dtype=torch.bfloat16) * 0.05)(H) * (H ** -0.5)),
        "b2": _w((lambda *s: torch.randn(*s, dtype=torch.bfloat16) * 1.6)(1) * 0.01),
    }

def reference(idx, table, w_p, w1, b1, w2, b2):
    emb = table[idx]
    B = emb.shape[0]; F = emb.shape[1]; D = emb.shape[2]
    ip = torch.einsum('bid,bjd->bij', emb, emb)
    ip = ip.reshape(B, F * F)
    logit_p = ip @ w_p
    z = emb.reshape(B, F * D)
    h = _gelu(z @ w1 + b1)
    logit_d = h @ w2 + b2
    return logit_p + logit_d
```

```cpp
#include <hip/hip_runtime.h>
#include <hip/hip_bf16.h>
#include <stdint.h>
#include <type_traits>

typedef short bf16x8 __attribute__((ext_vector_type(8)));
typedef float f32x4 __attribute__((ext_vector_type(4)));
typedef int i32x4 __attribute__((ext_vector_type(4)));
typedef unsigned u32x4 __attribute__((ext_vector_type(4)));
#define LAS __attribute__((address_space(3)))

constexpr int NB = 16384, NF = 100, ND = 64, NH = 256;
constexpr int W1T_BYTES = NF * ND * NH * 2;
constexpr int WPT_BYTES = 28 * 64 * 16;
constexpr int WPT_OFF = W1T_BYTES;

constexpr int A_STAGE = 8192, A_NST = 6;
constexpr int P_OFF = A_STAGE * A_NST;
constexpr int P_WAVE = 7 * 1024;
constexpr int IDX_OFF = P_OFF + 4 * P_WAVE;
constexpr int RED_OFF = IDX_OFF + 64 * NF * 4;
constexpr int BW_OFF = RED_OFF + 5 * 64 * 4;
constexpr int LDS_BYTES = BW_OFF + 513 * 4 + 12;

__device__ __forceinline__ float bf2f(unsigned short h) { return __builtin_bit_cast(float, (unsigned)h << 16); }

__global__ __launch_bounds__(256) void prep_kernel(const unsigned short* __restrict__ w1, const unsigned short* __restrict__ wp,
                                                   char* __restrict__ ws) {
  __shared__ __attribute__((aligned(16))) unsigned short tile[32 * 264];
  const int tid = threadIdx.x, blk = blockIdx.x;
  if (blk < 200) {
    const u32x4* src = (const u32x4*)(w1 + (size_t)blk * 32 * NH);
#pragma unroll
    for (int i = 0; i < 4; ++i) {
      int c = tid + 256 * i, r = c >> 5, cc = c & 31;
      *(u32x4*)(tile + r * 264 + cc * 8) = src[c];
    }
    __syncthreads();
    const int f = blk >> 1, ks = blk & 1;
#pragma unroll
    for (int i = 0; i < 4; ++i) {
      int c = tid + 256 * i, nt = c >> 6, l = c & 63;
      unsigned short v[8];
#pragma unroll
      for (int j = 0; j < 8; ++j) v[j] = tile[(8 * (l >> 4) + j) * 264 + nt * 16 + (l & 15)];
      u32x4 o;
#pragma unroll
      for (int j = 0; j < 4; ++j) o[j] = (unsigned)v[2 * j] | ((unsigned)v[2 * j + 1] << 16);
      *(u32x4*)(ws + (size_t)f * 32768 + (nt >> 2) * 8192 + ks * 4096 + (nt & 3) * 1024 + l * 16) = o;
    }
  } else {
    int c = (blk - 200) * 256 + tid;
    if (c < 28 * 64) {
      int t = c >> 6, l = c & 63;
      int q = 0; while ((q + 1) * (q + 2) / 2 <= t) ++q;
      int u = t - q * (q + 1) / 2;
      f32x4 o;
#pragma unroll
      for (int r = 0; r < 4; ++r) {
        int i = 16 * u + 4 * (l >> 4) + r, j = 16 * q + (l & 15);
        float v = 0.f;
        if (i < NF && j < NF) { v = bf2f(wp[i * NF + j]); if (u != q) v += bf2f(wp[j * NF + i]); }
        o[r] = v;
      }
      *(f32x4*)(ws + WPT_OFF + c * 16) = o;
    }
  }
}

__device__ __forceinline__ void dma16(unsigned voff, i32x4 srd, unsigned lds_dst) {
  asm volatile("s_mov_b32 m0, %0\n\ts_nop 0\n\tbuffer_load_dwordx4 %1, %2, 0 offen lds" :: "s"(lds_dst), "v"(voff), "s"(srd) : "memory");
}
template <int N> __device__ __forceinline__ void waitv() { asm volatile("s_waitcnt vmcnt(%0)" :: "i"(N) : "memory"); }
__device__ __forceinline__ void bar() {
  __builtin_amdgcn_sched_barrier(0);
  asm volatile("s_waitcnt lgkmcnt(0)" ::: "memory");
  __builtin_amdgcn_s_barrier();
  asm volatile("" ::: "memory");
  __builtin_amdgcn_sched_barrier(0);
}
__device__ __forceinline__ i32x4 make_srd(const void* p, unsigned bytes) {
  unsigned long long a = (unsigned long long)p;
  i32x4 r;
  r[0] = __builtin_amdgcn_readfirstlane((int)(unsigned)a);
  r[1] = __builtin_amdgcn_readfirstlane((int)(unsigned)(a >> 32) & 0xffff);
  r[2] = (int)bytes;
  r[3] = 0x00020000;
  return r;
}
#define MFMA(a, b, c) __builtin_amdgcn_mfma_f32_16x16x32_bf16(__builtin_bit_cast(__bf16 __attribute__((ext_vector_type(8))), a), __builtin_bit_cast(__bf16 __attribute__((ext_vector_type(8))), b), c, 0, 0, 0)

struct WSet { u32x4 r[8]; };
__device__ __forceinline__ void w_issue(WSet& s, unsigned voff, i32x4 srd, unsigned soff) {
  unsigned soff2 = soff + 4096;
  asm volatile("buffer_load_dwordx4 %0, %1, %2, %3 offen" : "=v"(s.r[0]) : "v"(voff), "s"(srd), "s"(soff) : "memory");
  asm volatile("buffer_load_dwordx4 %0, %1, %2, %3 offen offset:1024" : "=v"(s.r[1]) : "v"(voff), "s"(srd), "s"(soff) : "memory");
  asm volatile("buffer_load_dwordx4 %0, %1, %2, %3 offen offset:2048" : "=v"(s.r[2]) : "v"(voff), "s"(srd), "s"(soff) : "memory");
  asm volatile("buffer_load_dwordx4 %0, %1, %2, %3 offen offset:3072" : "=v"(s.r[3]) : "v"(voff), "s"(srd), "s"(soff) : "memory");
  asm volatile("buffer_load_dwordx4 %0, %1, %2, %3 offen" : "=v"(s.r[4]) : "v"(voff), "s"(srd), "s"(soff2) : "memory");
  asm volatile("buffer_load_dwordx4 %0, %1, %2, %3 offen offset:1024" : "=v"(s.r[5]) : "v"(voff), "s"(srd), "s"(soff2) : "memory");
  asm volatile("buffer_load_dwordx4 %0, %1, %2, %3 offen offset:2048" : "=v"(s.r[6]) : "v"(voff), "s"(srd), "s"(soff2) : "memory");
  asm volatile("buffer_load_dwordx4 %0, %1, %2, %3 offen offset:3072" : "=v"(s.r[7]) : "v"(voff), "s"(srd), "s"(soff2) : "memory");
}
template <int N> __device__ __forceinline__ void w_wait(WSet& s) {
  asm volatile("s_waitcnt vmcnt(%8)"
               : "+v"(s.r[0]), "+v"(s.r[1]), "+v"(s.r[2]), "+v"(s.r[3]), "+v"(s.r[4]), "+v"(s.r[5]), "+v"(s.r[6]), "+v"(s.r[7])
               : "i"(N) : "memory");
}

__device__ __forceinline__ float wave_sum(float v) {
  v += __shfl_xor(v, 1); v += __shfl_xor(v, 2); v += __shfl_xor(v, 4); v += __shfl_xor(v, 8);
  v += __shfl_xor(v, 16); v += __shfl_xor(v, 32);
  return v;
}

__global__ __launch_bounds__(512, 2) void main_kernel(const int* __restrict__ idx, const char* __restrict__ table, unsigned long long table_bytes,
                                                      const char* __restrict__ ws, const unsigned short* __restrict__ b1,
                                                      const unsigned short* __restrict__ w2, const unsigned short* __restrict__ b2,
                                                      __hip_bfloat16* __restrict__ out) {
  extern __shared__ __attribute__((aligned(16))) char lds_raw[];
  LAS char* lds = (LAS char*)lds_raw;
  const int tid = threadIdx.x, lane = tid & 63, wid = __builtin_amdgcn_readfirstlane(tid >> 6);
  const int sbase = blockIdx.x * 64;
  const unsigned lds0 = (unsigned)(size_t)lds;
  const int r16 = lane & 15, g4 = lane >> 4;
  const unsigned frag0 = r16 * 128 + ((g4 ^ ((r16 >> 1) & 7)) << 4);
  const unsigned frag1 = frag0 ^ 64;

  {
    const i32x4* src = (const i32x4*)(idx + (size_t)sbase * NF);
    for (int i = tid; i < 64 * 25; i += 512) *(LAS i32x4*)(lds + IDX_OFF + i * 16) = src[i];
  }
  LAS float* red = (LAS float*)(lds + RED_OFF);
  LAS float* bw = (LAS float*)(lds + BW_OFF);
  bw[tid] = bf2f(tid < 256 ? b1[tid] : w2[tid - 256]);
  if (tid == 0) bw[512] = bf2f(b2[0]);

  if (wid < 4) {
    const i32x4 srdW = make_srd(ws, W1T_BYTES);
    const unsigned voffW = lane * 16;
    const unsigned wbase = wid * 8192;
    f32x4 acc[4][4];
#pragma unroll
    for (int m = 0; m < 4; ++m)
#pragma unroll
      for (int n = 0; n < 4; ++n) acc[m][n] = f32x4{0.f, 0.f, 0.f, 0.f};
    WSet w0, w1, w2s;
    w_issue(w0, voffW, srdW, wbase);
    w_issue(w1, voffW, srdW, wbase + 32768);
    bar();
    bar();
    auto step = [&](WSet& cur, WSet& nxt, int n, int slot, bool issue, auto waitc) {
      if (issue) w_issue(nxt, voffW, srdW, wbase + (n + 2) * 32768);
      u32x4 a[4][2];
#pragma unroll
      for (int m = 0; m < 4; ++m) {
        a[m][0] = *(LAS u32x4*)(lds + slot * A_STAGE + m * 2048 + frag0);
        a[m][1] = *(LAS u32x4*)(lds + slot * A_STAGE + m * 2048 + frag1);
      }
      w_wait<decltype(waitc)::value>(cur);
#pragma unroll
      for (int k = 0; k < 2; ++k)
#pragma unroll
        for (int m = 0; m < 4; ++m)
#pragma unroll
          for (int nn = 0; nn < 4; ++nn) acc[m][nn] = MFMA(cur.r[k * 4 + nn], a[m][k], acc[m][nn]);
      bar();
    };
    using I16 = std::integral_constant<int, 16>;
    using I8 = std::integral_constant<int, 8>;
    using I0 = std::integral_constant<int, 0>;
    for (int n = 0; n < 96; n += 6) {
      step(w0, w2s, n + 0, 0, true, I16{});
      step(w1, w0, n + 1, 1, true, I16{});
      step(w2s, w1, n + 2, 2, true, I16{});
      step(w0, w2s, n + 3, 3, true, I16{});
      step(w1, w0, n + 4, 4, true, I16{});
      step(w2s, w1, n + 5, 5, true, I16{});
    }
    step(w0, w2s, 96, 0, true, I16{});
    step(w1, w0, 97, 1, true, I16{});
    step(w2s, w1, 98, 2, false, I8{});
    step(w0, w2s, 99, 3, false, I0{});
    f32x4 bb[4], ww[4];
#pragma unroll
    for (int nn = 0; nn < 4; ++nn) {
      bb[nn] = *(LAS f32x4*)(bw + wid * 64 + nn * 16 + g4 * 4);
      ww[nn] = *(LAS f32x4*)(bw + 256 + wid * 64 + nn * 16 + g4 * 4);
    }
#pragma unroll
    for (int m = 0; m < 4; ++m) {
      float s = 0.f;
#pragma unroll
      for (int nn = 0; nn < 4; ++nn)
#pragma unroll
        for (int r = 0; r < 4; ++r) {
          float x = acc[m][nn][r] + bb[nn][r];
          float t = __builtin_fmaf(x * x, -0.1029432397f, -2.302208198f);
          float e = __builtin_amdgcn_exp2f(x * t);
          float g = x * __builtin_amdgcn_rcpf(1.f + e);
          s = __builtin_fmaf(g, ww[nn][r], s);
        }
      s += __shfl_xor(s, 16);
      s += __shfl_xor(s, 32);
      if (lane < 16) red[wid * 64 + m * 16 + lane] = s;
    }
  } else {
    const int p = wid - 4;
    const i32x4 srdT = make_srd(table, (unsigned)table_bytes);
    f32x4 wp[28];
    {
      const f32x4* src = (const f32x4*)(ws + WPT_OFF) + lane;
#pragma unroll
      for (int t = 0; t < 28; ++t) wp[t] = src[t * 64];
#pragma unroll
      for (int t = 0; t < 28; ++t) asm volatile("" : "+v"(wp[t]));
    }
    const int r8 = lane >> 3;
    const unsigned chE = (unsigned)(((lane & 7) ^ (lane >> 4)) << 4);
    const unsigned chO = chE ^ 64;
    LAS const int* ixA = (LAS const int*)(lds + IDX_OFF) + (16 * p + r8) * NF;
    LAS const int* ixP = (LAS const int*)(lds + IDX_OFF) + 16 * p * NF + r8;
    const unsigned ldsA = lds0 + p * 2048, ldsP = lds0 + P_OFF + p * P_WAVE;
    LAS const char* myP = lds + P_OFF + p * P_WAVE;
    const unsigned fragT0 = (r16 & 3) * 128 + ((g4 ^ ((r16 >> 1) & 1)) << 4), fragT1 = fragT0 ^ 64;
    const int r8t = r8 & 3;

    auto issueA = [&](int f, int slot) {
      int i0 = ixA[f], i1 = ixA[8 * NF + f];
      dma16(((unsigned)i0 << 7) + chE, srdT, ldsA + slot * A_STAGE);
      dma16(((unsigned)i1 << 7) + chO, srdT, ldsA + slot * A_STAGE + 1024);
    };
    auto issueP = [&](int s, int k) {
      int i0 = ixP[s * NF + 16 * k], i1 = ixP[s * NF + 16 * k + 8];
      dma16(((unsigned)i0 << 7) + chE, srdT, ldsP + (k % 3) * 2048);
      dma16(((unsigned)i1 << 7) + chO, srdT, ldsP + (k % 3) * 2048 + 1024);
      if (k == 5) {
        int i2 = ixP[s * NF + 96 - r8 + r8t];
        dma16(((unsigned)i2 << 7) + chE, srdT, ldsP + 6144);
      }
    };
    asm volatile("s_waitcnt vmcnt(0)" ::: "memory");
    bar();
    issueA(0, 0);
    issueA(1, 1); issueP(0, 0);
    issueA(2, 2); issueP(0, 1);
    waitv<8>();
    bar();

    u32x4 fr[7][2];
    f32x4 ps;
    auto gram = [&](auto qc) {
      constexpr int q = decltype(qc)::value;
#pragma unroll
      for (int u = 0; u <= q; ++u) {
        f32x4 gacc = f32x4{0.f, 0.f, 0.f, 0.f};
        gacc = MFMA(fr[u][0], fr[q][0], gacc);
        gacc = MFMA(fr[u][1], fr[q][1], gacc);
        ps += gacc * wp[q * (q + 1) / 2 + u];
      }
    };
    auto step = [&](int s, auto qc, auto lastc) {
      constexpr int q = decltype(qc)::value;
      constexpr bool last = decltype(lastc)::value;
      const int n = 6 * s + q;
      if constexpr (last && q == 5) waitv<2>();
      else if constexpr (q == 4) waitv<5>();
      else waitv<4>();
      issueA(n + 3, (q + 3) % 6);
      if constexpr (q + 2 < 6) issueP(s, q + 2);
      else if constexpr (!last) issueP(s + 1, q + 2 - 6);
      if constexpr (q == 0) ps = f32x4{0.f, 0.f, 0.f, 0.f};
      fr[q][0] = *(LAS const u32x4*)(myP + (q % 3) * 2048 + frag0);
      fr[q][1] = *(LAS const u32x4*)(myP + (q % 3) * 2048 + frag1);
      if constexpr (q == 5) {
        fr[6][0] = *(LAS const u32x4*)(myP + 6144 + fragT0);
        fr[6][1] = *(LAS const u32x4*)(myP + 6144 + fragT1);
      }
      gram(qc);
      if constexpr (q == 5) {
        gram(std::integral_constant<int, 6>{});
        float tot = wave_sum((ps[0] + ps[1]) + (ps[2] + ps[3]));
        if (lane == 0) red[256 + 16 * p + s] = tot;
      } else {
        asm volatile("" : "+v"(ps));
      }
      bar();
    };
#define PSTEPS(S, L) \
    step(S, std::integral_constant<int, 0>{}, L); step(S, std::integral_constant<int, 1>{}, L); \
    step(S, std::integral_constant<int, 2>{}, L); step(S, std::integral_constant<int, 3>{}, L); \
    step(S, std::integral_constant<int, 4>{}, L); step(S, std::integral_constant<int, 5>{}, L);
    for (int s = 0; s < 15; ++s) { PSTEPS(s, std::false_type{}) }
    PSTEPS(15, std::true_type{})
    waitv<2>(); issueA(99, 3); bar();
    waitv<2>(); bar();
    waitv<0>(); bar();
    bar();
  }
  bar();
  if (tid < 64) {
    float v = red[256 + tid] + ((red[tid] + red[64 + tid]) + (red[128 + tid] + red[192 + tid])) + bw[512];
    out[sbase + tid] = __float2bfloat16(v);
  }
}

extern "C" void kernel_launch(void* const* d_in, const int* in_sizes, int n_in, void* d_out, int out_size, void* d_ws, size_t ws_size,
                              hipStream_t stream) {
  const int* idx = (const int*)d_in[0];
  const char* table = (const char*)d_in[1];
  const unsigned short* wp = (const unsigned short*)d_in[2];
  const unsigned short* w1 = (const unsigned short*)d_in[3];
  const unsigned short* b1 = (const unsigned short*)d_in[4];
  const unsigned short* w2 = (const unsigned short*)d_in[5];
  const unsigned short* b2 = (const unsigned short*)d_in[6];
  (void)hipFuncSetAttribute((const void*)main_kernel, hipFuncAttributeMaxDynamicSharedMemorySize, LDS_BYTES);
  prep_kernel<<<207, 256, 0, stream>>>(w1, wp, (char*)d_ws);
  main_kernel<<<NB / 64, 512, LDS_BYTES, stream>>>(idx, table, (unsigned long long)in_sizes[1] * 2ull, (const char*)d_ws, b1, w2, b2,
                                                   (__hip_bfloat16*)d_out);
}
```

```cpp
#include <hip/hip_runtime.h>
#include <hip/hip_bf16.h>
#include <stdint.h>
#include <type_traits>

typedef float f32x4 __attribute__((ext_vector_type(4)));
typedef int i32x4 __attribute__((ext_vector_type(4)));
typedef int i32x2 __attribute__((ext_vector_type(2)));
typedef unsigned u32x4 __attribute__((ext_vector_type(4)));
typedef __bf16 bf16x8 __attribute__((ext_vector_type(8)));
#define LAS __attribute__((address_space(3)))
#define GAS __attribute__((address_space(1)))

constexpr int NB = 16384, NF = 100, ND = 64, NH = 256, HF = 50;
constexpr int NWG = 256;
#define DLY 8
#define BURST 0
#define PNN_PRIO 1
static_assert((HF - DLY) % 3 == 0 && DLY >= 2, "PNN start delay");
constexpr int W1T_BYTES = NF * ND * NH * 2;
constexpr int WPT_OFF = W1T_BYTES;
constexpr int FLAG_OFF = WPT_OFF + 32768;
constexpr int SLAB_OFF = FLAG_OFF + NWG * 256;
constexpr int SLAB_BYTES = 65536;
constexpr int A_STAGE = 16384;
constexpr int P_OFF = 4 * A_STAGE;
constexpr int P_WAVE = 13 * 1024;
constexpr int IDXO_OFF = P_OFF + 4 * P_WAVE;
constexpr int IDXP_OFF = IDXO_OFF + 64 * NF * 4;
constexpr int RED_OFF = IDXP_OFF + 64 * HF * 4;
constexpr int BW_OFF = RED_OFF + 5 * 64 * 4;
constexpr int LDS_BYTES = BW_OFF + 516 * 4;
static_assert(LDS_BYTES <= 160 * 1024, "LDS");

__device__ __forceinline__ float bf2f(unsigned short h) { return __builtin_bit_cast(float, (unsigned)h << 16); }

__global__ __launch_bounds__(256) void prep_kernel(const unsigned short* __restrict__ w1, const unsigned short* __restrict__ wp,
                                                   const char* __restrict__ table, const char* __restrict__ idx,
                                                   char* __restrict__ ws, unsigned long long table_bytes) {
  __shared__ __attribute__((aligned(16))) unsigned short tile[32 * 264];
  const int tid = threadIdx.x, blk = blockIdx.x;
  if (blk < 200) {
    const u32x4* src = (const u32x4*)(w1 + (size_t)blk * 32 * NH);
    u32x4 t[4];
#pragma unroll
    for (int i = 0; i < 4; ++i) t[i] = src[tid + 256 * i];
#pragma unroll
    for (int i = 0; i < 4; ++i) {
      int c = tid + 256 * i, r = c >> 5, cc = c & 31;
      *(u32x4*)(tile + r * 264 + cc * 8) = t[i];
    }
    __syncthreads();
    const int f = blk >> 1, ks = blk & 1;
#pragma unroll
    for (int i = 0; i < 4; ++i) {
      int c = tid + 256 * i, nt = c >> 6, l = c & 63;
      unsigned short v[8];
#pragma unroll
      for (int j = 0; j < 8; ++j) v[j] = tile[(8 * (l >> 4) + j) * 264 + nt * 16 + (l & 15)];
      u32x4 o;
#pragma unroll
      for (int j = 0; j < 4; ++j) o[j] = (unsigned)v[2 * j] | ((unsigned)v[2 * j + 1] << 16);
      *(u32x4*)(ws + (size_t)f * 32768 + (nt >> 2) * 8192 + ks * 4096 + (nt & 3) * 1024 + l * 16) = o;
    }
  } else if (blk < 228) {
    const int t = blk - 200, l = tid >> 2, r = tid & 3;
    int q = 0; while ((q + 1) * (q + 2) / 2 <= t) ++q;
    const int u = t - q * (q + 1) / 2;
    const int i = 16 * u + 4 * (l >> 4) + r, j = 16 * q + (l & 15);
    const bool ok = i < NF && j < NF;
    const float a = bf2f(wp[ok ? i * NF + j : 0]), b = bf2f(wp[ok ? j * NF + i : 0]);
    *(float*)(ws + WPT_OFF + t * 1024 + tid * 4) = ok ? (u != q ? a + b : a) : 0.f;
  } else if (blk == 228) {
    *(unsigned*)(ws + FLAG_OFF + tid * 256) = 0u;
  }

}

__device__ __forceinline__ void dma16(unsigned voff, i32x4 srd, unsigned lds_dst) {
  asm volatile("s_mov_b32 m0, %0\n\ts_nop 0\n\tbuffer_load_dwordx4 %1, %2, 0 offen lds" :: "s"(lds_dst), "v"(voff), "s"(srd) : "memory");
}
template <int N> __device__ __forceinline__ void waitv() { asm volatile("s_waitcnt vmcnt(%0)" :: "i"(N) : "memory"); }
__device__ __forceinline__ void bar() {
  __builtin_amdgcn_sched_barrier(0);
  asm volatile("s_waitcnt lgkmcnt(0)" ::: "memory");
  __builtin_amdgcn_s_barrier();
  asm volatile("" ::: "memory");
  __builtin_amdgcn_sched_barrier(0);
}
__device__ __forceinline__ void bar_nw() {
  __builtin_amdgcn_sched_barrier(0);
  __builtin_amdgcn_s_barrier();
  asm volatile("" ::: "memory");
  __builtin_amdgcn_sched_barrier(0);
}
__device__ __forceinline__ i32x4 make_srd(const void* p, unsigned bytes) {
  unsigned long long a = (unsigned long long)p;
  i32x4 r;
  r[0] = __builtin_amdgcn_readfirstlane((int)(unsigned)a);
  r[1] = __builtin_amdgcn_readfirstlane((int)(unsigned)(a >> 32) & 0xffff);
  r[2] = (int)bytes;
  r[3] = 0x00020000;
  return r;
}
#define MFMA(a, b, c) __builtin_amdgcn_mfma_f32_16x16x32_bf16(__builtin_bit_cast(bf16x8, a), __builtin_bit_cast(bf16x8, b), c, 0, 0, 0)
#define IC(x) std::integral_constant<int, x>{}

struct WSet { u32x4 r[4]; };
template <int J> __device__ __forceinline__ void w_ld(WSet& s, unsigned voff, i32x4 srd, unsigned soff) {
  asm volatile("buffer_load_dwordx4 %0, %1, %2, %3 offen offset:%4" : "=v"(s.r[J]) : "v"(voff), "s"(srd), "s"(soff), "i"(J * 1024) : "memory");
}
template <int N> __device__ __forceinline__ void w_wait(WSet& s) {
  asm volatile("s_waitcnt vmcnt(%4)" : "+v"(s.r[0]), "+v"(s.r[1]), "+v"(s.r[2]), "+v"(s.r[3]) : "i"(N) : "memory");
}
template <int CTRL, int RM> __device__ __forceinline__ float dpp_add(float v) {
  return v + __builtin_bit_cast(float, __builtin_amdgcn_update_dpp(0, __builtin_bit_cast(int, v), CTRL, RM, 0xf, true));
}
__device__ __forceinline__ float wave_sum63(float v) {
  v = dpp_add<0x111, 0xf>(v); v = dpp_add<0x112, 0xf>(v); v = dpp_add<0x114, 0xf>(v); v = dpp_add<0x118, 0xf>(v);
  v = dpp_add<0x142, 0xa>(v); v = dpp_add<0x143, 0xc>(v);
  return v;
}
#define STAMP(i)

__global__ __launch_bounds__(512, 2) void main_kernel(const int* __restrict__ idx, const char* __restrict__ table, unsigned long long table_bytes,
                                                      char* __restrict__ ws, const unsigned short* __restrict__ b1,
                                                      const unsigned short* __restrict__ w2, const unsigned short* __restrict__ b2,
                                                      __hip_bfloat16* __restrict__ out) {
  extern __shared__ __attribute__((aligned(16))) char lds_raw[];
  LAS char* lds = (LAS char*)lds_raw;
  const int tid = threadIdx.x, lane = tid & 63, wid = __builtin_amdgcn_readfirstlane(tid >> 6);
  const int bid = blockIdx.x, h = (bid >> 3) & 1;
  const int pair = (bid & 7) * 16 + (bid >> 4);
  const int sown = pair * 128 + 64 * h, spar = pair * 128 + 64 * (h ^ 1);
  const unsigned lds0 = (unsigned)(size_t)lds;
  const int r16 = lane & 15, g4 = lane >> 4;
  const unsigned frag0 = r16 * 128 + ((g4 ^ ((r16 >> 1) & 7)) << 4);
  const unsigned frag1 = frag0 ^ 64;
  LAS float* red = (LAS float*)(lds + RED_OFF);
  LAS float* bw = (LAS float*)(lds + BW_OFF);

  {
    const i32x4* so = (const i32x4*)(idx + (size_t)sown * NF);
    const int* sp = idx + (size_t)spar * NF + HF * h;
    i32x4 to[4]; i32x2 tp[4];
#pragma unroll
    for (int i = 0; i < 4; ++i) {
      const int c = tid + 512 * i, cc = c < 1600 ? c : 0;
      to[i] = so[cc];
      tp[i] = *(const i32x2*)(sp + (cc / 25) * NF + (cc % 25) * 2);
    }
    const float bv = bf2f(tid < 256 ? b1[tid] : w2[tid - 256]), b2v = bf2f(b2[0]);
#pragma unroll
    for (int i = 0; i < 4; ++i) {
      const int c = tid + 512 * i;
      if (c < 1600) { *(LAS i32x4*)(lds + IDXO_OFF + c * 16) = to[i]; *(LAS i32x2*)(lds + IDXP_OFF + c * 8) = tp[i]; }
    }
    bw[tid] = bv;
    if (tid == 0) { bw[512] = b2v; bw[513] = 0.f; }
  }

  if (wid < 4) {
    const i32x4 srdW = make_srd(ws, W1T_BYTES);
    const unsigned voffW = lane * 16;
    const unsigned wbase = h * (HF * 32768) + wid * 8192;
    f32x4 acc[8][4];
#pragma unroll
    for (int m = 0; m < 8; ++m)
#pragma unroll
      for (int n = 0; n < 4; ++n) acc[m][n] = f32x4{0.f, 0.f, 0.f, 0.f};
    WSet w0, w1, w2s, w3;
#define W_LD4(S, OFF) w_ld<0>(S, voffW, srdW, OFF); w_ld<1>(S, voffW, srdW, OFF); w_ld<2>(S, voffW, srdW, OFF); w_ld<3>(S, voffW, srdW, OFF);
    W_LD4(w0, wbase) W_LD4(w1, wbase + 4096) W_LD4(w2s, wbase + 32768)
    bar();
    bar();
    u32x4 a[4];
    unsigned b0 = frag0, b1a = frag1;
#pragma unroll
    for (int m = 0; m < 4; ++m) a[m] = *(LAS u32x4*)(lds + b0 + m * 2048);
    auto kstep = [&](WSet& cur, WSet& nxt, unsigned soff, unsigned cb, auto coc, unsigned nb, auto noc, auto issuec, auto waitc) {
      constexpr int co = decltype(coc)::value, no = decltype(noc)::value;
      constexpr bool issue = decltype(issuec)::value;
      w_wait<decltype(waitc)::value>(cur);
      __builtin_amdgcn_sched_barrier(0);
#define GRP(M) \
      if constexpr (issue && (M & 1) == 0) w_ld<M / 2>(nxt, voffW, srdW, soff); \
      GEMM_MFMAS(M) \
      __builtin_amdgcn_sched_barrier(0); \
      if constexpr (M < 4) a[M & 3] = *(LAS u32x4*)(lds + cb + co + (M + 4) * 2048); \
      else if constexpr (no >= 0) a[M & 3] = *(LAS u32x4*)(lds + nb + no + (M - 4) * 2048); \
      __builtin_amdgcn_sched_barrier(0);
#define GEMM_MFMAS(M) _Pragma("unroll") for (int nn = 0; nn < 4; ++nn) acc[M][nn] = MFMA(cur.r[nn], a[M & 3], acc[M][nn]);
      GRP(0) GRP(1) GRP(2) GRP(3) GRP(4) GRP(5) GRP(6) GRP(7)
#undef GRP
    };
    using T = std::true_type; using Fa = std::false_type;
    unsigned so = wbase + 32768 + 4096;
    for (int n = 0; n < 48; n += 2) {
      kstep(w0, w3, so, b0, IC(0), b1a, IC(0), T{}, IC(8));
      kstep(w1, w0, so + 28672, b1a, IC(0), b0, IC(A_STAGE), T{}, IC(8));
      STAMP(0) bar_nw(); STAMP(3)
      kstep(w2s, w1, so + 32768, b0, IC(A_STAGE), b1a, IC(A_STAGE), T{}, IC(8));
      b0 ^= 2 * A_STAGE;
      kstep(w3, w2s, so + 32768 + 28672, b1a, IC(A_STAGE), b0, IC(0), T{}, IC(8));
      b1a ^= 2 * A_STAGE;
      STAMP(0) bar_nw(); STAMP(3)
      so += 65536;
    }
    kstep(w0, w3, so, b0, IC(0), b1a, IC(0), T{}, IC(8));
    kstep(w1, w0, 0, b1a, IC(0), b0, IC(A_STAGE), Fa{}, IC(8));
    kstep(w2s, w1, 0, b0, IC(A_STAGE), b1a, IC(A_STAGE), Fa{}, IC(4));
    kstep(w3, w2s, 0, b1a, IC(A_STAGE), b0, IC(-1), Fa{}, IC(0));
    {
      const __amdgpu_buffer_rsrc_t rs = __builtin_amdgcn_make_buffer_rsrc(ws + SLAB_OFF + (size_t)bid * SLAB_BYTES, 0, SLAB_BYTES, 0x00020000);
#pragma unroll
      for (int m = 0; m < 4; ++m)
#pragma unroll
        for (int nn = 0; nn < 4; ++nn)
          __builtin_amdgcn_raw_buffer_store_b128(__builtin_bit_cast(u32x4, acc[4 + m][nn]), rs, wid * 16384 + (m * 4 + nn) * 1024 + lane * 16, 0, 16);
      asm volatile("s_waitcnt vmcnt(0)" ::: "memory");
      if (lane == 0) {
        const unsigned old = __hip_atomic_fetch_add((LAS unsigned*)(bw + 513), 1u, __ATOMIC_RELAXED, __HIP_MEMORY_SCOPE_WORKGROUP);
        if (old == 3) __hip_atomic_store((GAS unsigned*)(ws + FLAG_OFF + bid * 256), 1u, __ATOMIC_RELAXED, __HIP_MEMORY_SCOPE_AGENT);
      }
      GAS unsigned* pf = (GAS unsigned*)(ws + FLAG_OFF + (bid ^ 8) * 256);
      for (unsigned spins = 0; spins < (1u << 22); ++spins) {
        if (__builtin_amdgcn_readfirstlane(__hip_atomic_load(pf, __ATOMIC_RELAXED, __HIP_MEMORY_SCOPE_AGENT)) == 1) break;
        __builtin_amdgcn_s_sleep(1);
      }
      asm volatile("" ::: "memory");
    }
    {
      const __amdgpu_buffer_rsrc_t rs = __builtin_amdgcn_make_buffer_rsrc(ws + SLAB_OFF + (size_t)(bid ^ 8) * SLAB_BYTES, 0, SLAB_BYTES, 0x00020000);
#pragma unroll
      for (int m = 0; m < 4; ++m)
#pragma unroll
        for (int nn = 0; nn < 4; ++nn)
          acc[4 + m][nn] = __builtin_bit_cast(f32x4, __builtin_amdgcn_raw_buffer_load_b128(rs, wid * 16384 + (m * 4 + nn) * 1024 + lane * 16, 0, 16));
    }
    f32x4 bb[4], ww[4];
#pragma unroll
    for (int nn = 0; nn < 4; ++nn) {
      bb[nn] = *(LAS f32x4*)(bw + wid * 64 + nn * 16 + g4 * 4);
      ww[nn] = *(LAS f32x4*)(bw + 256 + wid * 64 + nn * 16 + g4 * 4);
    }
#pragma unroll
    for (int m = 0; m < 4; ++m) {
      float s = 0.f;
#pragma unroll
      for (int nn = 0; nn < 4; ++nn)
#pragma unroll
        for (int r = 0; r < 4; ++r) {
          float x = (acc[m][nn][r] + acc[4 + m][nn][r]) + bb[nn][r];
          float t = __builtin_fmaf(x * x, -0.1029432397f, -2.302208198f);
          float e = __builtin_amdgcn_exp2f(x * t);
          float g = x * __builtin_amdgcn_rcpf(1.f + e);
          s = __builtin_fmaf(g, ww[nn][r], s);
        }
      s += __shfl_xor(s, 16);
      s += __shfl_xor(s, 32);
      if (lane < 16) red[wid * 64 + m * 16 + lane] = s;
    }
  } else {
    const int p = wid - 4;
    const i32x4 srdT = make_srd(table, (unsigned)table_bytes);
    f32x4 wp[28];
    {
      const f32x4* src = (const f32x4*)(ws + WPT_OFF) + lane;
#pragma unroll
      for (int t = 0; t < 28; ++t) wp[t] = src[t * 64];
#pragma unroll
      for (int t = 0; t < 28; ++t) asm volatile("" : "+v"(wp[t]));
    }
    const int r8 = lane >> 3;
    const unsigned chE = (unsigned)(((lane & 7) ^ (lane >> 4)) << 4);
    const unsigned chO = chE ^ 64;
    LAS const int* ixO = (LAS const int*)(lds + IDXO_OFF) + (16 * p + r8) * NF + HF * h;
    LAS const int* ixQ = (LAS const int*)(lds + IDXP_OFF) + (16 * p + r8) * HF;
    LAS const int* ixP = (LAS const int*)(lds + IDXO_OFF) + 16 * p * NF + r8;
    const unsigned ldsA = lds0 + p * 2048, ldsP = lds0 + P_OFF + p * P_WAVE;
    LAS const char* myP = lds + P_OFF + p * P_WAVE;
    const unsigned fragT0 = (r16 & 3) * 128 + ((g4 ^ ((r16 >> 1) & 1)) << 4), fragT1 = fragT0 ^ 64;
    const int r8t = r8 & 3;

    int ia[4], ip[5];
    auto ldA = [&](int j) { ia[0] = ixO[j]; ia[1] = ixO[8 * NF + j]; ia[2] = ixQ[j]; ia[3] = ixQ[8 * HF + j]; };
    auto ldP = [&](int s, auto gc) {
      constexpr int g = decltype(gc)::value;
#pragma unroll
      for (int i = 0; i < 4; ++i) ip[i] = ixP[s * NF + 32 * g + 8 * i];
      if constexpr (g == 2) ip[4] = ixP[s * NF + 96 - r8 + r8t];
    };
#define SB __builtin_amdgcn_sched_barrier(0);
#define DA(J, I) { SB dma16(((unsigned)ia[I] << 7) + ((I & 1) ? chO : chE), srdT, ldsA + ((J) & 3) * A_STAGE + (I >> 1) * 8192 + (I & 1) * 1024); SB }
#define DP(G, I) { SB dma16(((unsigned)ip[I] << 7) + ((I & 1) ? chO : chE), srdT, ldsP + G * 4096 + I * 1024); SB }
#define DA4(J) DA(J, 0) DA(J, 1) DA(J, 2) DA(J, 3)
#define DP4(G) DP(G, 0) DP(G, 1) DP(G, 2) DP(G, 3)
    __builtin_amdgcn_s_setprio(PNN_PRIO);
    bar();
    ldA(0); DA4(0) ldA(1); DA4(1) ldA(2); DA4(2)
    ldA(3);
    waitv<4>();
    bar();
    for (int n = 0; n < DLY - 2; ++n) { DA4(n + 3) ldA(n + 4); waitv<4>(); bar(); }
    ldP(0, IC(0)); DA4(DLY + 1) DP4(0) ldA(DLY + 2); ldP(0, IC(1)); waitv<8>(); bar();
    DA4(DLY + 2) DP4(1) ldA(DLY + 3); ldP(0, IC(2)); waitv<8>(); bar();

    u32x4 fr[7][2];
#pragma unroll
    for (int t = 4; t < 7; ++t) { fr[t][0] = u32x4{0, 0, 0, 0}; fr[t][1] = u32x4{0, 0, 0, 0}; }
    float ps0 = 0.f, ps1 = 0.f, ps2 = 0.f, ps3 = 0.f;
    auto pairq = [&](auto uc, auto qc) {
      constexpr int u = decltype(uc)::value, q = decltype(qc)::value;
      f32x4 gacc = f32x4{0.f, 0.f, 0.f, 0.f};
      gacc = MFMA(fr[u][0], fr[q][0], gacc);
      gacc = MFMA(fr[u][1], fr[q][1], gacc);
      const f32x4 wv = wp[q * (q + 1) / 2 + u];
      ps0 = __builtin_fmaf(gacc[0], wv[0], ps0); ps1 = __builtin_fmaf(gacc[1], wv[1], ps1);
      ps2 = __builtin_fmaf(gacc[2], wv[2], ps2); ps3 = __builtin_fmaf(gacc[3], wv[3], ps3);
    };
#define PQ(U, Q) pairq(IC(U), IC(Q));
    auto total = [&](int s) {
      asm("" : "+v"(ps0)); asm("" : "+v"(ps1)); asm("" : "+v"(ps2)); asm("" : "+v"(ps3));
      float tot = wave_sum63(((ps0 + ps1) + ps2) + ps3);
      if (lane == 63 && s >= 0) red[256 + 16 * p + s] = tot;
      ps0 = ps1 = ps2 = ps3 = 0.f;
    };
    auto rd = [&](auto tc) {
      constexpr int t = decltype(tc)::value;
      fr[t][0] = *(LAS const u32x4*)(myP + t * 2048 + (t == 6 ? fragT0 : frag0));
      fr[t][1] = *(LAS const u32x4*)(myP + t * 2048 + (t == 6 ? fragT1 : frag1));
    };
    auto steps = [&](int s, auto fac, auto nbarc, auto lastc) {
      constexpr bool fa = decltype(fac)::value, last = decltype(lastc)::value;
      constexpr int nbar = decltype(nbarc)::value;
      const int n = 3 * s + DLY;
#define XA(J, I) if constexpr (fa) DA(J, I)
#define XP(G, I) if constexpr (!last) DP(G, I)
#define ENDSTEP(CNT, K) { float x0 = ps0, x1 = ps1, x2 = ps2, x3 = ps3; asm volatile("" : "+v"(x0), "+v"(x1), "+v"(x2), "+v"(x3)); ps0 = x0; ps1 = x1; ps2 = x2; ps3 = x3; } STAMP(1) waitv<CNT>(); STAMP(2) if constexpr (nbar > K) bar(); else { SB asm volatile("s_waitcnt lgkmcnt(0)" ::: "memory"); SB } STAMP(3)
      rd(IC(0)); rd(IC(1));
      XA(n + 3, 0) DP(2, 0) PQ(4, 4) PQ(4, 5) XA(n + 3, 1) DP(2, 1) PQ(5, 5) PQ(4, 6) XA(n + 3, 2) DP(2, 2) PQ(5, 6) PQ(6, 6)
      total(s - 1);
      XA(n + 3, 3) DP(2, 3) PQ(0, 0) PQ(0, 1) PQ(1, 1) DP(2, 4)
      if constexpr (fa) ldA(n + 4);
      if constexpr (!last) ldP(s + 1, IC(0));
      ENDSTEP((fa ? 9 : 5), 0)
      rd(IC(2)); rd(IC(3));
      XA(n + 4, 0) XP(0, 0) XA(n + 4, 1) PQ(0, 2) PQ(1, 2) XP(0, 1) XA(n + 4, 2) PQ(2, 2) PQ(0, 3) XP(0, 2) XA(n + 4, 3) PQ(1, 3) PQ(2, 3) PQ(3, 3) XP(0, 3)
      if constexpr (fa) ldA(n + 5);
      if constexpr (!last) ldP(s + 1, IC(1));
      ENDSTEP(((fa ? 4 : 0) + (last ? 0 : 4)), 1)
      rd(IC(4)); rd(IC(5)); rd(IC(6));
      XA(n + 5, 0) XP(1, 0) PQ(0, 4) PQ(1, 4) XA(n + 5, 1) PQ(2, 4) PQ(3, 4) XP(1, 1) PQ(0, 5) PQ(1, 5) XA(n + 5, 2) PQ(2, 5) PQ(3, 5) XP(1, 2)
      PQ(0, 6) PQ(1, 6) XA(n + 5, 3) PQ(2, 6) PQ(3, 6) XP(1, 3)
      if constexpr (fa) ldA(n + 6 < HF ? n + 6 : 0);
      if constexpr (!last) ldP(s + 1, IC(2));
      ENDSTEP(((fa ? 4 : 0) + (last ? 0 : 4)), 2)
    };
    using T = std::true_type; using Fa = std::false_type;
    constexpr int S2 = (HF - DLY) / 3;
    for (int s = 0; s < S2 - 1; ++s) steps(s, T{}, IC(3), Fa{});
    if constexpr (S2 == 16) steps(15, Fa{}, IC(1), T{});
    else {
      steps(S2 - 1, Fa{}, IC(1), Fa{});
      for (int s = S2; s < 15; ++s) steps(s, Fa{}, IC(0), Fa{});
      steps(15, Fa{}, IC(0), T{});
    }
    PQ(4, 4) PQ(4, 5) PQ(5, 5) PQ(4, 6) PQ(5, 6) PQ(6, 6)
    total(15);
  }
  bar();
  if (tid < 64) {
    float v = red[256 + tid] + ((red[tid] + red[64 + tid]) + (red[128 + tid] + red[192 + tid])) + bw[512];
    out[sown + tid] = __float2bfloat16(v);
  }
}

extern "C" void kernel_launch(void* const* d_in, const int* in_sizes, int n_in, void* d_out, int out_size, void* d_ws, size_t ws_size,
                              hipStream_t stream) {
  const int* idx = (const int*)d_in[0];
  const char* table = (const char*)d_in[1];
  const unsigned short* wp = (const unsigned short*)d_in[2];
  const unsigned short* w1 = (const unsigned short*)d_in[3];
  const unsigned short* b1 = (const unsigned short*)d_in[4];
  const unsigned short* w2 = (const unsigned short*)d_in[5];
  const unsigned short* b2 = (const unsigned short*)d_in[6];
  const unsigned long long tb = (unsigned long long)in_sizes[1] * 2ull;
  (void)hipFuncSetAttribute((const void*)main_kernel, hipFuncAttributeMaxDynamicSharedMemorySize, LDS_BYTES);
  prep_kernel<<<256, 256, 0, stream>>>(w1, wp, table, (const char*)idx, (char*)d_ws, tb);
  main_kernel<<<NWG, 512, LDS_BYTES, stream>>>(idx, table, tb, (char*)d_ws, b1, w2, b2, (__hip_bfloat16*)d_out);
}
```

```cpp
#include <hip/hip_runtime.h>
#include <hip/hip_bf16.h>
#include <stdint.h>
#include <type_traits>

typedef float f32x4 __attribute__((ext_vector_type(4)));
typedef int i32x4 __attribute__((ext_vector_type(4)));
typedef int i32x2 __attribute__((ext_vector_type(2)));
typedef unsigned u32x4 __attribute__((ext_vector_type(4)));
typedef unsigned u32x2 __attribute__((ext_vector_type(2)));
typedef __bf16 bf16x8 __attribute__((ext_vector_type(8)));
typedef unsigned long long u64;
#define LAS __attribute__((address_space(3)))
#define GAS __attribute__((address_space(1)))

constexpr int NB = 16384, NF = 100, ND = 64, NH = 256, HF = 50;
constexpr int NWG = 256;
#define POST 1
#define NS 6
#define PKILL 0u
#define PM_PRIO 1
#define PF_PRIO 0
#define PNN_PRIO 1
constexpr int MAINS = 46, MID = 8, FB = 54;
constexpr int S2 = 16 - POST;
constexpr int G0 = HF - 3 * S2;
static_assert(NS % 2 == 0 && NS >= 4 && G0 >= 2, "schedule");
struct TailOp { int kind, a, n; };
constexpr TailOp tail_op(int u, int g) {
  if (u == 0) return g < 2 ? TailOp{1, 12 + 2 * g, 2} : TailOp{2, 4 * (g - 2), 4};
  if (u == 1) return g < 2 ? TailOp{2, 8 + 4 * g, 4} : TailOp{1, 16 + 2 * (g - 2), 2};
  if (u <= 4) return TailOp{1, 4 * (2 * u + 1) + 2 * g, 2};
  if (u == 6 || u == 7) return TailOp{1, 4 * (2 * u - 1) + 2 * g, 2};
  if (u == 8) return TailOp{1, 4 * 15 + g, 1};
  if (u >= 12) return TailOp{3, 4 * (u - 12) + g, 1};
  return TailOp{0, 0, 0};
}
constexpr int FLAG_U = 6;
constexpr int tail_wait(int u) {
  if (u == FLAG_U) return 0;
  int cnt = u < 3 ? 4 * (2 - u) : 0;
  for (int v = 0; v < u; ++v) {
    if (v == FLAG_U) ++cnt;
    for (int g = 0; g < 4; ++g) {
      const TailOp o = tail_op(v, g);
      if (o.kind == 1 && o.a + o.n - 1 == 4 * u + 3) cnt = 0; else cnt += o.n;
    }
  }
  return cnt;
}
constexpr int W1T_BYTES = NF * ND * NH * 2;
constexpr int EPOCH_OFF = W1T_BYTES;
constexpr int WFLAG_OFF = EPOCH_OFF + 1024;
constexpr int XFLAG_OFF = WFLAG_OFF + 8192;
constexpr int SLAB_OFF = XFLAG_OFF + NWG * 256;
constexpr int SLAB_BYTES = 65536;
constexpr int A_STAGE = 16384;
constexpr int P_OFF = 4 * A_STAGE;
constexpr int P_WAVE = 13 * 1024;
constexpr int IDXO_OFF = P_OFF + 4 * P_WAVE;
constexpr int IDXP_OFF = IDXO_OFF + 64 * NF * 4;
constexpr int QP = 48;
constexpr int RED_OFF = IDXP_OFF + 64 * QP * 4;
constexpr int BW_OFF = RED_OFF + 5 * 64 * 4;
constexpr int TOK_OFF = BW_OFF + 512 * 4;
constexpr int LDS_BYTES = TOK_OFF + 32;
static_assert(LDS_BYTES <= 160 * 1024, "LDS");

__device__ __forceinline__ float bf2f(unsigned short h) { return __builtin_bit_cast(float, (unsigned)h << 16); }
__device__ __forceinline__ void dma16(unsigned voff, i32x4 srd, unsigned lds_dst) {
  asm volatile("s_mov_b32 m0, %0\n\ts_nop 0\n\tbuffer_load_dwordx4 %1, %2, 0 offen lds" :: "s"(lds_dst), "v"(voff), "s"(srd) : "memory");
}
template <int N> __device__ __forceinline__ void waitv() { asm volatile("s_waitcnt vmcnt(%0)" :: "i"(N) : "memory"); }
#define SB __builtin_amdgcn_sched_barrier(0);
__device__ __forceinline__ void bar() {
  SB asm volatile("s_waitcnt lgkmcnt(0)" ::: "memory");
  __builtin_amdgcn_s_barrier();
  asm volatile("" ::: "memory"); SB
}
__device__ __forceinline__ void bar_nw() {
  SB __builtin_amdgcn_s_barrier();
  asm volatile("" ::: "memory"); SB
}
__device__ __forceinline__ i32x4 make_srd(const void* p, unsigned bytes) {
  u64 a = (u64)p;
  i32x4 r;
  r[0] = __builtin_amdgcn_readfirstlane((int)(unsigned)a);
  r[1] = __builtin_amdgcn_readfirstlane((int)(unsigned)(a >> 32) & 0xffff);
  r[2] = (int)bytes;
  r[3] = 0x00020000;
  return r;
}
#define MFMA(a, b, c) __builtin_amdgcn_mfma_f32_16x16x32_bf16(__builtin_bit_cast(bf16x8, a), __builtin_bit_cast(bf16x8, b), c, 0, 0, 0)
#define IC(x) std::integral_constant<int, x>{}
#define RLX_AGENT __ATOMIC_RELAXED, __HIP_MEMORY_SCOPE_AGENT

struct WSet { u32x4 r[4]; };
template <int J> __device__ __forceinline__ void w_ld(WSet& s, unsigned voff, i32x4 srd, unsigned soff) {
  asm volatile("buffer_load_dwordx4 %0, %1, %2, %3 offen offset:%4 sc1" : "=v"(s.r[J]) : "v"(voff), "s"(srd), "s"(soff), "i"(J * 1024) : "memory");
}
template <int N> __device__ __forceinline__ void w_wait(WSet& s) {
  asm volatile("s_waitcnt vmcnt(%4)" : "+v"(s.r[0]), "+v"(s.r[1]), "+v"(s.r[2]), "+v"(s.r[3]) : "i"(N) : "memory");
}
template <int J> __device__ __forceinline__ void x_st(const f32x4& v, unsigned voff, i32x4 srd, unsigned soff) {
  asm volatile("buffer_store_dwordx4 %0, %1, %2, %3 offen offset:%4 sc1" :: "v"(v), "v"(voff), "s"(srd), "s"(soff), "i"(J * 1024) : "memory");
}
template <int J> __device__ __forceinline__ void x_ld(f32x4& v, unsigned voff, i32x4 srd, unsigned soff) {
  asm volatile("buffer_load_dwordx4 %0, %1, %2, %3 offen offset:%4 sc1" : "=v"(v) : "v"(voff), "s"(srd), "s"(soff), "i"(J * 1024) : "memory");
}
template <int N> __device__ __forceinline__ void x_wait(f32x4 (&c)[4]) {
  asm volatile("s_waitcnt vmcnt(%4)" : "+v"(c[0]), "+v"(c[1]), "+v"(c[2]), "+v"(c[3]) : "i"(N) : "memory");
}
__device__ __forceinline__ void mfma1(f32x4& c, const u32x4& w, const u32x4& e) {
  asm volatile("v_mfma_f32_16x16x32_bf16 %0, %1, %2, %0" : "+v"(c) : "v"(w), "v"(e));
}
__device__ __forceinline__ void touch(const u32x4& x) { asm volatile("" :: "v"(x)); }
struct NSet { u32x2 r[8]; };
template <int J> __device__ __forceinline__ void n_ld(NSet& s, unsigned voff, i32x4 srd, unsigned soff) {
  asm volatile("buffer_load_dwordx2 %0, %1, %2, %3 offen offset:%4" : "=v"(s.r[J]) : "v"(voff), "s"(srd), "s"(soff), "i"(J * 512) : "memory");
}
template <int N> __device__ __forceinline__ void n_wait(NSet& s) {
  asm volatile("s_waitcnt vmcnt(%8)"
               : "+v"(s.r[0]), "+v"(s.r[1]), "+v"(s.r[2]), "+v"(s.r[3]), "+v"(s.r[4]), "+v"(s.r[5]), "+v"(s.r[6]), "+v"(s.r[7]) : "i"(N) : "memory");
}
__device__ __forceinline__ void n_frag(const NSet& s, WSet& o) {
#pragma unroll
  for (int c = 0; c < 4; ++c)
#pragma unroll
    for (int i = 0; i < 4; ++i) o.r[c][i] = __builtin_amdgcn_perm(s.r[2 * i + 1][c >> 1], s.r[2 * i][c >> 1], (c & 1) ? 0x07060302u : 0x05040100u);
}
#define N_LD8(S, V, OFF) n_ld<0>(S, V, srdN, OFF); n_ld<1>(S, V, srdN, OFF); n_ld<2>(S, V, srdN, OFF); n_ld<3>(S, V, srdN, OFF); \
                         n_ld<4>(S, V, srdN, OFF); n_ld<5>(S, V, srdN, OFF); n_ld<6>(S, V, srdN, OFF); n_ld<7>(S, V, srdN, OFF);
template <int CTRL, int RM> __device__ __forceinline__ float dpp_add(float v) {
  return v + __builtin_bit_cast(float, __builtin_amdgcn_update_dpp(0, __builtin_bit_cast(int, v), CTRL, RM, 0xf, true));
}
__device__ __forceinline__ float wave_sum63(float v) {
  v = dpp_add<0x111, 0xf>(v); v = dpp_add<0x112, 0xf>(v); v = dpp_add<0x114, 0xf>(v); v = dpp_add<0x118, 0xf>(v);
  v = dpp_add<0x142, 0xa>(v); v = dpp_add<0x143, 0xc>(v);
  return v;
}
__device__ __forceinline__ void sweep2(__amdgpu_buffer_rsrc_t rs, unsigned off, unsigned tlo, unsigned thi) {
  for (unsigned spins = 0; spins < (1u << 18); ++spins) {
    asm volatile("" ::: "memory");
    const u32x4 v = __builtin_amdgcn_raw_buffer_load_b128(rs, off, 0, 16);
    if (__all(v[0] == tlo && v[1] == thi && v[2] == tlo && v[3] == thi)) break;
    __builtin_amdgcn_s_sleep(2);
  }
  asm volatile("" ::: "memory");
}
#define TL(i)
#define STAMP(i)

__global__ __launch_bounds__(512, 2) void pnn_kernel(const int* __restrict__ idx, const char* __restrict__ table, u64 table_bytes,
                                                     const char* __restrict__ wpg, const char* __restrict__ w1,
                                                     const unsigned short* __restrict__ b1, const unsigned short* __restrict__ w2,
                                                     const unsigned short* __restrict__ b2, char* __restrict__ ws,
                                                     __hip_bfloat16* __restrict__ out) {
  extern __shared__ __attribute__((aligned(16))) char lds_raw[];
  LAS char* lds = (LAS char*)lds_raw;
  const int tid = threadIdx.x, lane = tid & 63, wid = __builtin_amdgcn_readfirstlane(tid >> 6);
  const int bid = blockIdx.x, h = (bid >> 3) & 1;
  const int pair = (bid & 7) * 16 + (bid >> 4);
  const int sown = pair * 128 + 64 * h, spar = pair * 128 + 64 * (h ^ 1);
  const unsigned lds0 = (unsigned)(size_t)lds;
  const int r16 = lane & 15, g4 = lane >> 4;
  const unsigned frag0 = r16 * 128 + ((g4 ^ ((r16 >> 1) & 7)) << 4);
  const unsigned frag1 = frag0 ^ 64;
  LAS float* red = (LAS float*)(lds + RED_OFF);
  using T = std::true_type; using Fa = std::false_type;
  const __amdgpu_buffer_rsrc_t rsW = __builtin_amdgcn_make_buffer_rsrc(ws, 0, SLAB_OFF, 0x00020000);
  const i32x4 srdN = make_srd(w1, W1T_BYTES);

  if (wid < 4) {
    LAS float* bw = (LAS float*)(lds + BW_OFF);
    const unsigned voffN = g4 * 4096 + wid * 128 + r16 * 8;
    const unsigned nbase = h * (FB * 32768);
    NSet n0, n1, n2, n3;
    N_LD8(n0, voffN, nbase) N_LD8(n1, voffN, nbase + 16384) N_LD8(n2, voffN, nbase + 32768)
    const i32x4 srdW = make_srd(ws, W1T_BYTES);
    const unsigned voffW = lane * 16;
    const unsigned wbase = h * (FB * 32768) + wid * 8192;
    WSet w0, w1s, w2s, w3;
#define W_LD4(S, OFF) w_ld<0>(S, voffW, srdW, OFF); w_ld<1>(S, voffW, srdW, OFF); w_ld<2>(S, voffW, srdW, OFF); w_ld<3>(S, voffW, srdW, OFF);
    f32x4 acc[8][4];
#pragma unroll
    for (int m = 0; m < 8; ++m)
#pragma unroll
      for (int n = 0; n < 4; ++n) { acc[m][n] = f32x4{0.f, 0.f, 0.f, 0.f}; asm volatile("" : "+v"(acc[m][n])); }
    TL(5)
    bar();
    TL(6)
    u32x4 a[4];
    unsigned b0 = frag0, b1a = frag1;
#pragma unroll
    for (int m = 0; m < 3; ++m) a[m] = *(LAS u32x4*)(lds + b0 + m * 2048);
#define GRP(CUR, M, RD, LD) \
      mfma1(acc[M][0], CUR.r[0], a[M & 3]); SB RD SB \
      mfma1(acc[M][1], CUR.r[1], a[M & 3]); LD \
      mfma1(acc[M][2], CUR.r[2], a[M & 3]); touch(a[(M + 1) & 3]); \
      mfma1(acc[M][3], CUR.r[3], a[M & 3]); SB
#define RDK(M) if constexpr (M < 5) a[(M + 3) & 3] = *(LAS u32x4*)(lds + cb + co + (M + 3) * 2048); \
               else a[(M + 3) & 3] = *(LAS u32x4*)(lds + nb + no + (M - 5) * 2048);
    auto kstep = [&](WSet& cur, WSet& nxt, unsigned soff, unsigned cb, auto coc, unsigned nb, auto noc) {
      constexpr int co = decltype(coc)::value, no = decltype(noc)::value;
      w_wait<8>(cur);
      SB
#define G(M) GRP(cur, M, RDK(M), if constexpr ((M & 1) == 0) w_ld<M / 2>(nxt, voffW, srdW, soff);)
      G(0) G(1) G(2) G(3) G(4) G(5) G(6) G(7)
#undef G
    };
    auto nstep = [&](NSet& curn, NSet& nn_, WSet& nw, auto natc, auto waitc, unsigned soff, unsigned cb, auto coc, unsigned nb, auto noc) {
      constexpr int co = decltype(coc)::value, no = decltype(noc)::value;
      constexpr bool nat = decltype(natc)::value;
      n_wait<decltype(waitc)::value>(curn);
      WSet cur; n_frag(curn, cur);
      SB asm volatile("s_nop 1"); SB
#define G(M) GRP(cur, M, RDK(M), if constexpr (nat) n_ld<M>(nn_, voffN, srdN, soff); else if constexpr ((M & 1) == 0) w_ld<M / 2>(nw, voffW, srdW, soff);)
      G(0) G(1) G(2) G(3) G(4) G(5) G(6) G(7)
#undef G
    };
    unsigned sn = nbase + 3 * 16384;
    for (int n = 0; n < NS - 2; n += 2) {
      nstep(n0, n3, w0, T{}, IC(16), sn, b0, IC(0), b1a, IC(0));
      nstep(n1, n0, w0, T{}, IC(16), sn + 16384, b1a, IC(0), b0, IC(A_STAGE));
      bar_nw();
      nstep(n2, n1, w0, T{}, IC(16), sn + 32768, b0, IC(A_STAGE), b1a, IC(A_STAGE));
      b0 ^= 2 * A_STAGE;
      nstep(n3, n2, w0, T{}, IC(16), sn + 49152, b1a, IC(A_STAGE), b0, IC(0));
      b1a ^= 2 * A_STAGE;
      bar_nw();
      sn += 65536;
    }
    nstep(n0, n3, w0, T{}, IC(16), sn, b0, IC(0), b1a, IC(0));
    nstep(n1, n0, w0, Fa{}, IC(16), wbase + NS * 32768, b1a, IC(0), b0, IC(A_STAGE));
    bar_nw();
    nstep(n2, n0, w1s, Fa{}, IC(12), wbase + NS * 32768 + 4096, b0, IC(A_STAGE), b1a, IC(A_STAGE));
    b0 ^= 2 * A_STAGE;
    nstep(n3, n0, w2s, Fa{}, IC(8), wbase + (NS + 1) * 32768, b1a, IC(A_STAGE), b0, IC(0));
    b1a ^= 2 * A_STAGE;
    bar_nw();
    unsigned so = wbase + (NS + 1) * 32768 + 4096;
    const unsigned wmid = MAINS * 32768 + wid * 8192;
    for (int n = NS; n < MAINS; n += 2) {
      kstep(w0, w3, so, b0, IC(0), b1a, IC(0));
      if (n == MAINS - 2) so = wmid - 28672;
      kstep(w1s, w0, so + 28672, b1a, IC(0), b0, IC(A_STAGE));
      STAMP(0) bar_nw(); STAMP(3)
      kstep(w2s, w1s, so + 32768, b0, IC(A_STAGE), b1a, IC(A_STAGE));
      b0 ^= 2 * A_STAGE;
      kstep(w3, w2s, so + 32768 + 28672, b1a, IC(A_STAGE), b0, IC(0));
      b1a ^= 2 * A_STAGE;
      STAMP(0) bar_nw(); STAMP(3)
      so += 65536;
    }
    {
      const i32x4 srdS = make_srd(ws + SLAB_OFF + (size_t)bid * SLAB_BYTES, SLAB_BYTES);
      const i32x4 srdR = make_srd(ws + SLAB_OFF + (size_t)(bid ^ 8) * SLAB_BYTES, SLAB_BYTES);
      const u64 tok = *(LAS const u64*)(lds + TOK_OFF + wid * 8);
      WSet ts[16];
      ts[0] = w0; ts[1] = w1s; ts[2] = w2s;
      auto top = [&](auto uc, auto gc, auto ic) {
        constexpr TailOp o = tail_op(decltype(uc)::value, decltype(gc)::value);
        constexpr int x = o.a + decltype(ic)::value;
        if constexpr (decltype(ic)::value < o.n) {
          if constexpr (o.kind == 1) w_ld<x & 3>(ts[x >> 2], voffW, srdW, wmid + (x >> 3) * 32768 + ((x >> 2) & 1) * 4096);
          if constexpr (o.kind == 2) x_st<x & 3>(acc[x >> 2][x & 3], voffW, srdS, wid * 16384 + (x >> 2) * 4096);
          if constexpr (o.kind == 3) x_ld<x & 3>(acc[x >> 2][x & 3], voffW, srdR, wid * 16384 + (x >> 2) * 4096);
        }
      };
      auto hk = [&](auto uc) {
        constexpr int U = decltype(uc)::value;
        constexpr int co = ((MAINS + U / 4) & 3) * A_STAGE + ((U >> 1) & 1) * 8192;
        constexpr int no = ((MAINS + (U + 1) / 4) & 3) * A_STAGE + (((U + 1) >> 1) & 1) * 8192;
        const unsigned cb = (U & 1) ? frag1 : frag0, nb = (U & 1) ? frag0 : frag1;
        WSet& cur = ts[U];
        w_wait<tail_wait(U)>(cur);
        if constexpr (U == FLAG_U) {
          if (lane == 0) __hip_atomic_store((GAS u64*)(ws + XFLAG_OFF + bid * 256 + wid * 64), tok, RLX_AGENT);
        }
        SB
#define RDR(M) if constexpr (M == 0) a[3] = *(LAS u32x4*)(lds + cb + co + 3 * 2048); \
               else if constexpr (U + 1 < 2 * MID) a[M - 1] = *(LAS u32x4*)(lds + nb + no + (M - 1) * 2048);
#define LDR(M) top(uc, IC(M), IC(0)); top(uc, IC(M), IC(1)); top(uc, IC(M), IC(2)); top(uc, IC(M), IC(3));
        GRP(cur, 4, RDR(0), LDR(0)) GRP(cur, 5, RDR(1), LDR(1)) GRP(cur, 6, RDR(2), LDR(2)) GRP(cur, 7, RDR(3), LDR(3))
      };
      hk(IC(0)); hk(IC(1)); hk(IC(2)); hk(IC(3)); bar_nw();
      hk(IC(4)); hk(IC(5)); hk(IC(6)); hk(IC(7)); bar_nw();
      hk(IC(8)); hk(IC(9)); hk(IC(10)); hk(IC(11)); bar_nw();
      hk(IC(12)); hk(IC(13)); hk(IC(14)); hk(IC(15));
      asm volatile("s_nop 7"); SB
    }
    f32x4 bb[4], ww[4];
#pragma unroll
    for (int r = 0; r < 4; ++r) {
      bb[r] = *(LAS f32x4*)(bw + wid * 64 + g4 * 16 + r * 4);
      ww[r] = *(LAS f32x4*)(bw + 256 + wid * 64 + g4 * 16 + r * 4);
    }
#pragma unroll
    for (int m = 0; m < 4; ++m) {
      float s = 0.f;
      if (m == 0) x_wait<12>(acc[0]); else if (m == 1) x_wait<8>(acc[1]); else if (m == 2) x_wait<4>(acc[2]); else x_wait<0>(acc[3]);
#pragma unroll
      for (int nn = 0; nn < 4; ++nn)
#pragma unroll
        for (int r = 0; r < 4; ++r) {
          float x = (acc[4 + m][nn][r] + bb[r][nn]) + acc[m][nn][r];
          float t = __builtin_fmaf(x * x, -0.1029432397f, -2.302208198f);
          float e = __builtin_amdgcn_exp2f(x * t);
          float g = x * __builtin_amdgcn_rcpf(1.f + e);
          s = __builtin_fmaf(g, ww[r][nn], s);
        }
      s += __shfl_xor(s, 16);
      s += __shfl_xor(s, 32);
      if (lane < 16) red[wid * 64 + m * 16 + lane] = s;
    }
  } else {
    const int p = wid - 4;
    const i32x4 srdT = make_srd(table, (unsigned)table_bytes);
    __builtin_amdgcn_s_setprio(PNN_PRIO);
    {
      const i32x4 srdI = make_srd(idx, NB * NF * 4);
      const unsigned oo = (unsigned)(sown + 16 * p) * 400u + lane * 16, op = (unsigned)(spar + 16 * p) * 400u + FB * 4u * h;
#pragma unroll
      for (int i = 0; i < 3; ++i) {
        const unsigned c = lane + 64 * i;
        dma16(op + (c / 12) * 400 + (c % 12) * 16, srdI, lds0 + IDXP_OFF + p * (16 * QP * 4) + i * 1024);
      }
#pragma unroll
      for (int i = 0; i < 7; ++i)
        if (i < 6 || lane < 16) dma16(oo + i * 1024, srdI, lds0 + IDXO_OFF + p * 6400 + i * 1024);
    }
    NSet raw; u32x2 ep;
    N_LD8(raw, g4 * 4096 + p * 128 + r16 * 8, (unsigned)(bid < 200 ? bid : 0) * 16384u)
    asm volatile("global_load_dwordx2 %0, %1, off sc1" : "=v"(ep) : "v"(ws + EPOCH_OFF + p * 256) : "memory");
    unsigned short bv, wv;
    asm volatile("global_load_ushort %0, %1, off" : "=v"(bv) : "v"(b1 + p * 64 + lane) : "memory");
    asm volatile("global_load_ushort %0, %1, off" : "=v"(wv) : "v"(w2 + p * 64 + lane) : "memory");
    {
      const i32x4 srdP = make_srd(wpg, NF * NF * 2);
#pragma unroll
      for (int i = 0; i < 5; ++i) dma16(p * 5000 + i * 1024 + lane * 16, srdP, lds0 + P_OFF + p * P_WAVE + 8192 + i * 1024);
      waitv<16>();
    }
    TL(1)
    const int r8 = lane >> 3;
    const unsigned chE = (unsigned)(((lane & 7) ^ (lane >> 4)) << 4);
    const unsigned chO = chE ^ 64;
    LAS const int* ixO = (LAS const int*)(lds + IDXO_OFF) + (16 * p + r8) * NF;
    LAS const int* ixQ = (LAS const int*)(lds + IDXP_OFF) + (16 * p + r8) * QP;
    LAS const int* ixP = (LAS const int*)(lds + IDXO_OFF) + 16 * p * NF + r8;
    const unsigned ldsA = lds0 + p * 2048, ldsP = lds0 + P_OFF + p * P_WAVE;
    LAS const char* myP = lds + P_OFF + p * P_WAVE;
    const int rt = r16 < 4 ? r16 : 4;
    const unsigned fragT0 = rt * 128 + ((g4 ^ (rt >> 1)) << 4), fragT1 = fragT0 ^ 64;
    const unsigned tailmask = r8 < 4 ? 0u : 0x80000000u;

    int ia[4], ip[5];
    auto ldA = [&](int j) {
      const bool t = j >= MAINS;
      LAS const int* lo = t ? ixO + (2 * j - MAINS) : ixQ + j;
      LAS const int* hi = ixO + (t ? 2 * j - MAINS + 1 : FB * h + j);
      ia[0] = lo[0]; ia[1] = lo[t ? 8 * NF : 8 * QP]; ia[2] = hi[0]; ia[3] = hi[8 * NF];
    };
    auto ldP = [&](int s, auto gc) {
      constexpr int g = decltype(gc)::value;
#pragma unroll
      for (int i = 0; i < 4; ++i) ip[i] = ixP[s * NF + 32 * g + 8 * i];
      if constexpr (g == 2) ip[4] = ixP[s * NF + 96 - (r8 & 4)];
    };
#define DA(J, I) { SB dma16((((unsigned)ia[I] << 7) + ((I & 1) ? chO : chE)), srdT, ldsA + ((J) & 3) * A_STAGE + (I >> 1) * 8192 + (I & 1) * 1024); SB }
#define DP(G, I) { SB dma16((((unsigned)ip[I] << 7) + ((I & 1) ? chO : chE)) | (I == 4 ? tailmask : 0u) | PKILL, srdT, ldsP + G * 4096 + I * 1024); SB }
#define DA4(J) DA(J, 0) DA(J, 1) DA(J, 2) DA(J, 3)
#define DP4(G) DP(G, 0) DP(G, 1) DP(G, 2) DP(G, 3)
    ldA(0); DA4(0) ldA(1); DA4(1) ldA(2); DA4(2)
    unsigned tlo, thi;
    {
      n_wait<12>(raw);
      asm volatile("" : "+v"(ep), "+v"(bv), "+v"(wv));
      WSet o; n_frag(raw, o);
      const unsigned dst = bid < 200 ? (bid >> 1) * 32768 + p * 8192 + (bid & 1) * 4096 + lane * 16 : 0x80000000u;
#pragma unroll
      for (int c = 0; c < 4; ++c) __builtin_amdgcn_raw_buffer_store_b128(o.r[c], rsW, dst + c * 1024, 0, 16);
      const u64 e = ((u64)(unsigned)__builtin_amdgcn_readfirstlane((int)ep[1]) << 32) | (unsigned)__builtin_amdgcn_readfirstlane((int)ep[0]);
      u64 tok = (e ^ 0x9E3779B97F4A7C15ull) * 0xBF58476D1CE4E5B9ull + 0x632BE59BD9B4E019ull;
      if (tok == e) tok = ~e;
      tlo = (unsigned)tok; thi = (unsigned)(tok >> 32);
      LAS float* bw = (LAS float*)(lds + BW_OFF);
      bw[p * 64 + lane] = bf2f(bv); bw[256 + p * 64 + lane] = bf2f(wv);
      if (lane == 0) *(LAS u64*)(lds + TOK_OFF + p * 8) = tok;
    }
    ldA(3);
    TL(2)
    waitv<8>();
    TL(3)
    bar();
    TL(4)
    DA4(3) ldA(4);
    f32x4 wp[28];
    {
      u32x4 R[7][4];
#pragma unroll
      for (int t = 0; t < 7; ++t) {
        const int i = min(16 * t + r16, NF - 1);
        LAS const char* rp = lds + P_OFF + (i / 25) * P_WAVE + 8192 + (i % 25) * 200 + 16 * g4;
#pragma unroll
        for (int kb = 0; kb < 4; ++kb) {
          const u32x2 lo = *(LAS const u32x2*)(rp + 64 * kb), hi = *(LAS const u32x2*)(rp + 64 * kb + 8);
          R[t][kb] = u32x4{lo[0], lo[1], hi[0], hi[1]};
        }
      }
      u32x4 sel[2];
#pragma unroll
      for (int x = 0; x < 2; ++x)
#pragma unroll
        for (int i = 0; i < 4; ++i) {
          const int d = 16 * x + r16 - 8 * g4 - 2 * i;
          sel[x][i] = d == 0 ? 0x00003F80u : d == 1 ? 0x3F800000u : 0u;
        }
#pragma unroll
      for (int q = 0; q < 7; ++q)
#pragma unroll
        for (int u = 0; u <= q; ++u) {
          f32x4 v = MFMA(R[u][q >> 1], sel[q & 1], (f32x4{0.f, 0.f, 0.f, 0.f}));
          if (u != q) v = MFMA(sel[u & 1], R[q][u >> 1], v);
          wp[q * (q + 1) / 2 + u] = v;
        }
#pragma unroll
      for (int t = 0; t < 28; ++t) asm volatile("" : "+v"(wp[t]));
    }
    waitv<0>();
    if (lane == 0) __hip_atomic_store((GAS u64*)(ws + WFLAG_OFF + p * 2048 + bid * 8), ((u64)thi << 32) | tlo, RLX_AGENT);
    TL(5)
    bar();

    u32x4 fr[7][2];
#pragma unroll
    for (int t = 4; t < 7; ++t) { fr[t][0] = u32x4{0, 0, 0, 0}; fr[t][1] = u32x4{0, 0, 0, 0}; }
    float ps0 = 0.f, ps1 = 0.f, ps2 = 0.f, ps3 = 0.f;
    f32x4 gq[2][2];
    auto pm = [&](auto cc, auto u0c, auto q0c, auto u1c, auto q1c) {
      constexpr int c = decltype(cc)::value, u0 = decltype(u0c)::value, q0 = decltype(q0c)::value, u1 = decltype(u1c)::value, q1 = decltype(q1c)::value;
      const f32x4 z = f32x4{0.f, 0.f, 0.f, 0.f};
      SB __builtin_amdgcn_s_setprio(PM_PRIO);
      gq[c][0] = MFMA(fr[u0][0], fr[q0][0], z);
      if constexpr (u1 >= 0) gq[c][1] = MFMA(fr[u1][0], fr[q1][0], z);
      gq[c][0] = MFMA(fr[u0][1], fr[q0][1], gq[c][0]);
      if constexpr (u1 >= 0) gq[c][1] = MFMA(fr[u1][1], fr[q1][1], gq[c][1]);
      SB __builtin_amdgcn_s_setprio(PF_PRIO); SB
    };
    auto pf = [&](auto cc, auto u0c, auto q0c, auto u1c, auto q1c) {
      constexpr int c = decltype(cc)::value, u0 = decltype(u0c)::value, q0 = decltype(q0c)::value, u1 = decltype(u1c)::value, q1 = decltype(q1c)::value;
      const f32x4 w0 = wp[q0 * (q0 + 1) / 2 + u0];
      ps0 = __builtin_fmaf(gq[c][0][0], w0[0], ps0); ps1 = __builtin_fmaf(gq[c][0][1], w0[1], ps1);
      ps2 = __builtin_fmaf(gq[c][0][2], w0[2], ps2); ps3 = __builtin_fmaf(gq[c][0][3], w0[3], ps3);
      if constexpr (u1 >= 0) {
        const f32x4 w1v = wp[q1 * (q1 + 1) / 2 + u1];
        ps0 = __builtin_fmaf(gq[c][1][0], w1v[0], ps0); ps1 = __builtin_fmaf(gq[c][1][1], w1v[1], ps1);
        ps2 = __builtin_fmaf(gq[c][1][2], w1v[2], ps2); ps3 = __builtin_fmaf(gq[c][1][3], w1v[3], ps3);
      }
      SB
    };
#define PM(C, U0, Q0, U1, Q1) pm(IC(C), IC(U0), IC(Q0), IC(U1), IC(Q1));
#define PF(C, U0, Q0, U1, Q1) pf(IC(C), IC(U0), IC(Q0), IC(U1), IC(Q1));
    auto total = [&](int s) {
      asm("" : "+v"(ps0)); asm("" : "+v"(ps1)); asm("" : "+v"(ps2)); asm("" : "+v"(ps3));
      float tot = wave_sum63(((ps0 + ps1) + ps2) + ps3);
      if (lane == 63 && s >= 0) red[256 + 16 * p + s] = tot;
      ps0 = ps1 = ps2 = ps3 = 0.f;
    };
    auto rd = [&](auto tc) {
      constexpr int t = decltype(tc)::value;
      fr[t][0] = *(LAS const u32x4*)(myP + t * 2048 + (t == 6 ? fragT0 : frag0));
      fr[t][1] = *(LAS const u32x4*)(myP + t * 2048 + (t == 6 ? fragT1 : frag1));
    };
    auto wsweep = [&](int n) {
      if (n == NS - 3) { sweep2(rsW, WFLAG_OFF + p * 2048 + h * (2 * MAINS * 8) + (lane < FB ? lane : FB - 1) * 16, tlo, thi); TL(6) }
    };
    static_assert(NS - 3 < G0 && NS - 3 >= 1, "sweep step");
    auto xpoll = [&]() {
      GAS u64* pf = (GAS u64*)(ws + XFLAG_OFF + (bid ^ 8) * 256 + p * 64);
      const u64 tok = ((u64)thi << 32) | tlo;
      for (unsigned spins = 0; spins < (1u << 18); ++spins) {
        if (__all(__hip_atomic_load(pf, RLX_AGENT) == tok)) break;
        __builtin_amdgcn_s_sleep(1);
      }
      asm volatile("" ::: "memory");
    };
    auto steps = [&](int s, auto fac, auto nbarc, auto lastc) {
      constexpr bool fa = decltype(fac)::value, last = decltype(lastc)::value;
      constexpr int nbar = decltype(nbarc)::value;
      const int n = 3 * s + G0;
#define XA(J, I) if constexpr (fa) DA(J, I)
#define XP(G, I) if constexpr (!last) DP(G, I)
#define ENDSTEP(CNT, K) { float x0 = ps0, x1 = ps1, x2 = ps2, x3 = ps3; asm volatile("" : "+v"(x0), "+v"(x1), "+v"(x2), "+v"(x3)); ps0 = x0; ps1 = x1; ps2 = x2; ps3 = x3; } \
      STAMP(1) waitv<CNT>(); STAMP(2) if constexpr (nbar == 2 && K == 1) xpoll(); if constexpr (nbar > K) bar(); else { SB asm volatile("s_waitcnt lgkmcnt(0)" ::: "memory"); SB } STAMP(3)
      rd(IC(0)); rd(IC(1));
      PM(0, 4, 4, 4, 5) XA(n + 3, 0) DP(2, 0)
      PM(1, 5, 5, 4, 6) PF(0, 4, 4, 4, 5) XA(n + 3, 1) DP(2, 1)
      PM(0, 5, 6, 6, 6) PF(1, 5, 5, 4, 6) XA(n + 3, 2) DP(2, 2)
      PM(1, 0, 0, 0, 1) PF(0, 5, 6, 6, 6) total(s - 1); XA(n + 3, 3) DP(2, 3)
      PM(0, 1, 1, -1, -1) PF(1, 0, 0, 0, 1) DP(2, 4) PF(0, 1, 1, -1, -1)
      if constexpr (fa) ldA(n + 4);
      if constexpr (!last) ldP(s + 1, IC(0));
      ENDSTEP((fa ? 9 : 5), 0)
      rd(IC(2)); rd(IC(3));
      XA(n + 4, 0) XP(0, 0)
      PM(0, 0, 2, 1, 2) XA(n + 4, 1) XP(0, 1)
      PM(1, 2, 2, 0, 3) PF(0, 0, 2, 1, 2) XA(n + 4, 2) XP(0, 2)
      PM(0, 1, 3, 2, 3) PF(1, 2, 2, 0, 3) XA(n + 4, 3)
      PM(1, 3, 3, -1, -1) PF(0, 1, 3, 2, 3) XP(0, 3) PF(1, 3, 3, -1, -1)
      if constexpr (fa) ldA(n + 5);
      if constexpr (!last) ldP(s + 1, IC(1));
      ENDSTEP(((fa ? 4 : 0) + (last ? 0 : 4)), 1)
      rd(IC(4)); rd(IC(5)); rd(IC(6));
      XA(n + 5, 0) XP(1, 0)
      PM(0, 0, 4, 1, 4) XA(n + 5, 1)
      PM(1, 2, 4, 3, 4) PF(0, 0, 4, 1, 4) XP(1, 1)
      PM(0, 0, 5, 1, 5) PF(1, 2, 4, 3, 4) XA(n + 5, 2)
      PM(1, 2, 5, 3, 5) PF(0, 0, 5, 1, 5) XP(1, 2)
      PM(0, 0, 6, 1, 6) PF(1, 2, 5, 3, 5) XA(n + 5, 3)
      PM(1, 2, 6, 3, 6) PF(0, 0, 6, 1, 6) XP(1, 3) PF(1, 2, 6, 3, 6)
      if constexpr (fa) ldA(n + 6 < HF ? n + 6 : 0);
      if constexpr (!last) ldP(s + 1, IC(2));
      ENDSTEP(((fa ? 4 : 0) + (last ? 0 : 4)), 2)
    };
    static_assert(G0 >= 3, "step 0 builds the weights, steps G0-2 and G0-1 start the PNN gathers");
    for (int n = 1; n < G0 - 2; ++n) { DA4(n + 3) ldA(n + 4); waitv<4>(); wsweep(n); bar(); }
    ldP(0, IC(0)); DA4(G0 + 1) DP4(0) ldA(G0 + 2); ldP(0, IC(1)); waitv<8>(); wsweep(G0 - 2); bar();
    DA4(G0 + 2) DP4(1) ldA(G0 + 3); ldP(0, IC(2)); waitv<8>(); wsweep(G0 - 1); bar();
    for (int s = 0; s < S2 - 1; ++s) steps(s, T{}, IC(3), Fa{});
    if constexpr (POST == 0) steps(15, Fa{}, IC(2), T{});
    else {
      steps(S2 - 1, Fa{}, IC(2), Fa{});
      for (int s = S2; s < 15; ++s) steps(s, Fa{}, IC(0), Fa{});
      steps(15, Fa{}, IC(0), T{});
    }
    PM(0, 4, 4, 4, 5) PM(1, 5, 5, 4, 6) PF(0, 4, 4, 4, 5) PM(0, 5, 6, 6, 6) PF(1, 5, 5, 4, 6) PF(0, 5, 6, 6, 6)
    total(15);
    if (bid == 0) {
      sweep2(rsW, WFLAG_OFF + p * 2048 + lane * 16, tlo, thi);
      sweep2(rsW, WFLAG_OFF + p * 2048 + 1024 + lane * 16, tlo, thi);
      if (lane == 0) __hip_atomic_store((GAS u64*)(ws + EPOCH_OFF + p * 256), ((u64)thi << 32) | tlo, RLX_AGENT);
    }
  }
  const float b2v = bf2f(b2[0]);
  bar();
  if (tid < 64) {
    float v = red[256 + tid] + ((red[tid] + red[64 + tid]) + (red[128 + tid] + red[192 + tid])) + b2v;
    out[sown + tid] = __float2bfloat16(v);
  }
}

extern "C" void kernel_launch(void* const* d_in, const int* in_sizes, int n_in, void* d_out, int out_size, void* d_ws, size_t ws_size,
                              hipStream_t stream) {
  (void)hipFuncSetAttribute((const void*)pnn_kernel, hipFuncAttributeMaxDynamicSharedMemorySize, LDS_BYTES);
  pnn_kernel<<<NWG, 512, LDS_BYTES, stream>>>((const int*)d_in[0], (const char*)d_in[1], (u64)in_sizes[1] * 2ull, (const char*)d_in[2],
                                              (const char*)d_in[3], (const unsigned short*)d_in[4], (const unsigned short*)d_in[5],
                                              (const unsigned short*)d_in[6], (char*)d_ws, (__hip_bfloat16*)d_out);
}
```

```cpp
#include <hip/hip_runtime.h>
#include <hip/hip_bf16.h>
#include <stdint.h>
#include <type_traits>

typedef float f32x4 __attribute__((ext_vector_type(4)));
typedef float f32x2 __attribute__((ext_vector_type(2)));
typedef int i32x4 __attribute__((ext_vector_type(4)));
typedef int i32x2 __attribute__((ext_vector_type(2)));
typedef unsigned u32x4 __attribute__((ext_vector_type(4)));
typedef unsigned u32x2 __attribute__((ext_vector_type(2)));
typedef __bf16 bf16x8 __attribute__((ext_vector_type(8)));
typedef unsigned long long u64;
#define LAS __attribute__((address_space(3)))
#define GAS __attribute__((address_space(1)))

constexpr int NB = 16384, NF = 100, ND = 64, NH = 256, HF = 50;
constexpr int NWG = 256;
#define POST 1
#define PHOLD 0
#define PKG 1
#define PSLEEP 8
#define PDELAY 12
#define HIDEF 0
#define TOTB 1
#define PKILL 0u
#define PM_PRIO 1
#define PF_PRIO 0
#define PNN_PRIO 1
#define MID 8
constexpr int MAINS = (NF - MID) / 2, FB = MAINS + MID, NU = 2 * MID;
constexpr int S2 = 16 - POST;
constexpr int G0 = HF - 3 * S2;
static_assert(G0 >= 2 && MAINS % 2 == 0 && MAINS <= 48, "schedule");
constexpr int FLAG_U = NU - 8;
constexpr int t_store(int u, int g) {
  if (MID >= 12) return g == 1 && u < 8 ? u : -1;
  return u < 3 ? ((g & 1) ? 2 * u + (g >> 1) : -1) : (u < 5 && g == 1 ? u + 3 : -1);
}
constexpr int t_load(int u, int g) { return u >= NU - 4 && (g & 1) ? 2 * (u - NU + 4) + (g >> 1) : -1; }
constexpr int t_iss(int v) {
  if (v < 3) return -1;
  if (v <= FLAG_U + 3) { const int x = FLAG_U - 2 - (FLAG_U + 3 - v) / 2; return v - 3 < x ? v - 3 : x; }
  return FLAG_U + (v - FLAG_U - 4) / 2;
}
constexpr int t_woff(int v) { return ((v >> 2) + ((v & 2) ? 0 : 4)) * 32768 + (v & 1) * 4096; }
constexpr int t_wn(int u) { int c = 0; for (int v = 3; v < NU; ++v) c += t_iss(v) == u; return c; }
constexpr int t_w0(int u) { for (int v = 3; v < NU; ++v) if (t_iss(v) == u) return 4 * v; return 0; }
constexpr int tail_wait(int u) {
  if (u == FLAG_U) return 0;
  int cnt = u < 3 ? 4 * (2 - u) : 0;
  for (int v = 0; v < u; ++v) {
    if (v == FLAG_U) ++cnt;
    for (int g = 0; g < 4; ++g) {
      cnt += (t_store(v, g) >= 0) + (t_load(v, g) >= 0);
      if (t_wn(v) > 0 && t_w0(v) + t_wn(v) * (g + 1) - 1 == 4 * u + 3) cnt = 0; else cnt += t_wn(v);
    }
  }
  return cnt;
}
constexpr int W1T_BYTES = NF * ND * NH * 2;
constexpr int EPOCH_OFF = W1T_BYTES;
constexpr int WFLAG_OFF = EPOCH_OFF + 1024;
constexpr int XFLAG_OFF = WFLAG_OFF + 8192;
constexpr int SLAB_OFF = XFLAG_OFF + NWG * 256;
constexpr int SLAB_BYTES = 32768;
constexpr int A_STAGE = 16384;
constexpr int P_OFF = 4 * A_STAGE;
constexpr int P_WAVE = 13 * 1024;
constexpr int WPL_OFF = 2 * A_STAGE;
constexpr int OP = 104;
constexpr int IDXO_OFF = P_OFF + 4 * P_WAVE;
constexpr int IDXP_OFF = IDXO_OFF + 64 * OP * 4;
constexpr int QP = 48;
constexpr int RED_OFF = IDXP_OFF + 64 * QP * 4;
constexpr int BW_OFF = RED_OFF + 5 * 64 * 4;
constexpr int TOK_OFF = BW_OFF + 260 * 4;
constexpr int LDS_BYTES = TOK_OFF + 32;
static_assert(LDS_BYTES <= 160 * 1024, "LDS");

__device__ __forceinline__ float bf2f(unsigned short h) { return __builtin_bit_cast(float, (unsigned)h << 16); }
__device__ __forceinline__ void dma16(unsigned voff, i32x4 srd, unsigned lds_dst) {
  asm volatile("s_mov_b32 m0, %0\n\ts_nop 0\n\tbuffer_load_dwordx4 %1, %2, 0 offen lds" :: "s"(lds_dst), "v"(voff), "s"(srd) : "memory");
}
template <int N> __device__ __forceinline__ void waitv() { asm volatile("s_waitcnt vmcnt(%0)" :: "i"(N) : "memory"); }
#define SB __builtin_amdgcn_sched_barrier(0);
__device__ __forceinline__ void bar() {
  SB asm volatile("s_waitcnt lgkmcnt(0)" ::: "memory");
  __builtin_amdgcn_s_barrier();
  asm volatile("" ::: "memory"); SB
}
__device__ __forceinline__ void bar_nw() {
  SB __builtin_amdgcn_s_barrier();
  asm volatile("" ::: "memory"); SB
}
__device__ __forceinline__ i32x4 make_srd(const void* p, unsigned bytes) {
  u64 a = (u64)p;
  i32x4 r;
  r[0] = __builtin_amdgcn_readfirstlane((int)(unsigned)a);
  r[1] = __builtin_amdgcn_readfirstlane((int)(unsigned)(a >> 32) & 0xffff);
  r[2] = (int)bytes;
  r[3] = 0x00020000;
  return r;
}
#define MFMA(a, b, c) __builtin_amdgcn_mfma_f32_16x16x32_bf16(__builtin_bit_cast(bf16x8, a), __builtin_bit_cast(bf16x8, b), c, 0, 0, 0)
#define IC(x) std::integral_constant<int, x>{}
#define RLX_AGENT __ATOMIC_RELAXED, __HIP_MEMORY_SCOPE_AGENT

struct WSet { u32x4 r[4]; };
template <int J> __device__ __forceinline__ void w_ld(WSet& s, unsigned voff, i32x4 srd, unsigned soff) {
  asm volatile("buffer_load_dwordx4 %0, %1, %2, %3 offen offset:%4 sc1" : "=v"(s.r[J]) : "v"(voff), "s"(srd), "s"(soff), "i"(J * 1024) : "memory");
}
template <int N> __device__ __forceinline__ void w_wait(WSet& s) {
  asm volatile("s_waitcnt vmcnt(%4)" : "+v"(s.r[0]), "+v"(s.r[1]), "+v"(s.r[2]), "+v"(s.r[3]) : "i"(N) : "memory");
}
template <int J> __device__ __forceinline__ void x_st(const u32x4& v, unsigned voff, i32x4 srd, unsigned soff) {
  asm volatile("buffer_store_dwordx4 %0, %1, %2, %3 offen offset:%4 sc1\n\ts_nop 1" :: "v"(v), "v"(voff), "s"(srd), "s"(soff), "i"(J * 1024) : "memory");
}
template <int J> __device__ __forceinline__ void x_ld(u32x4& v, unsigned voff, i32x4 srd, unsigned soff) {
  asm volatile("buffer_load_dwordx4 %0, %1, %2, %3 offen offset:%4 sc1" : "=v"(v) : "v"(voff), "s"(srd), "s"(soff), "i"(J * 1024) : "memory");
}
template <int N> __device__ __forceinline__ void x_wait(u32x4& c0, u32x4& c1) {
  asm volatile("s_waitcnt vmcnt(%2)" : "+v"(c0), "+v"(c1) : "i"(N) : "memory");
}
__device__ __forceinline__ unsigned pk_bf16(float lo, float hi) {
  typedef __bf16 bf16x2 __attribute__((ext_vector_type(2)));
  return __builtin_bit_cast(unsigned, bf16x2{(__bf16)lo, (__bf16)hi});
}
__device__ __forceinline__ void mfma1(f32x4& c, const u32x4& w, const u32x4& e) {
  asm volatile("v_mfma_f32_16x16x32_bf16 %0, %1, %2, %0" : "+v"(c) : "v"(w), "v"(e));
}
__device__ __forceinline__ void touch(const u32x4& x) { asm volatile("" :: "v"(x)); }
struct NSet { u32x2 r[8]; };
template <int J> __device__ __forceinline__ void n_ld(NSet& s, unsigned voff, i32x4 srd, unsigned soff) {
  asm volatile("buffer_load_dwordx2 %0, %1, %2, %3 offen offset:%4" : "=v"(s.r[J]) : "v"(voff), "s"(srd), "s"(soff), "i"(J * 512) : "memory");
}
template <int N> __device__ __forceinline__ void n_wait(NSet& s) {
  asm volatile("s_waitcnt vmcnt(%8)"
               : "+v"(s.r[0]), "+v"(s.r[1]), "+v"(s.r[2]), "+v"(s.r[3]), "+v"(s.r[4]), "+v"(s.r[5]), "+v"(s.r[6]), "+v"(s.r[7]) : "i"(N) : "memory");
}
__device__ __forceinline__ void n_frag(const NSet& s, WSet& o) {
#pragma unroll
  for (int c = 0; c < 4; ++c)
#pragma unroll
    for (int i = 0; i < 4; ++i) o.r[c][i] = __builtin_amdgcn_perm(s.r[2 * i + 1][c >> 1], s.r[2 * i][c >> 1], (c & 1) ? 0x07060302u : 0x05040100u);
}
#define N_LD8(S, V, OFF) n_ld<0>(S, V, srdN, OFF); n_ld<1>(S, V, srdN, OFF); n_ld<2>(S, V, srdN, OFF); n_ld<3>(S, V, srdN, OFF); \
                         n_ld<4>(S, V, srdN, OFF); n_ld<5>(S, V, srdN, OFF); n_ld<6>(S, V, srdN, OFF); n_ld<7>(S, V, srdN, OFF);
template <int CTRL, int RM> __device__ __forceinline__ float dpp_add(float v) {
  return v + __builtin_bit_cast(float, __builtin_amdgcn_update_dpp(0, __builtin_bit_cast(int, v), CTRL, RM, 0xf, true));
}
__device__ __forceinline__ float wave_sum63(float v) {
  v = dpp_add<0x111, 0xf>(v); v = dpp_add<0x112, 0xf>(v); v = dpp_add<0x114, 0xf>(v); v = dpp_add<0x118, 0xf>(v);
  v = dpp_add<0x142, 0xa>(v); v = dpp_add<0x143, 0xc>(v);
  return v;
}
__device__ __forceinline__ void sweep2(__amdgpu_buffer_rsrc_t rs, unsigned off, unsigned tlo, unsigned thi) {
  for (unsigned spins = 0; spins < (1u << 18); ++spins) {
    asm volatile("" ::: "memory");
    const u32x4 v = __builtin_amdgcn_raw_buffer_load_b128(rs, off, 0, 16);
    if (__all(v[0] == tlo && v[1] == thi && v[2] == tlo && v[3] == thi)) break;
    __builtin_amdgcn_s_sleep(PSLEEP);
  }
  asm volatile("" ::: "memory");
}
#define TL(i)
#define STAMP(i)

__global__ __launch_bounds__(512, 2) void pnn_kernel(const int* __restrict__ idx, const char* __restrict__ table, u64 table_bytes,
                                                     const char* __restrict__ wpg, const char* __restrict__ w1,
                                                     const unsigned short* __restrict__ b1, const unsigned short* __restrict__ w2,
                                                     const unsigned short* __restrict__ b2, char* __restrict__ ws,
                                                     __hip_bfloat16* __restrict__ out) {
  extern __shared__ __attribute__((aligned(16))) char lds_raw[];
  LAS char* lds = (LAS char*)lds_raw;
  const int tid = threadIdx.x, lane = tid & 63, wid = __builtin_amdgcn_readfirstlane(tid >> 6);
  const int bid = blockIdx.x, h = (bid >> 3) & 1;
  const int pair = (bid & 7) * 16 + (bid >> 4);
  const int sown = pair * 128 + 64 * h, spar = pair * 128 + 64 * (h ^ 1);
  const unsigned lds0 = (unsigned)(size_t)lds;
  const int r16 = lane & 15, g4 = lane >> 4;
  const unsigned frag0 = r16 * 128 + ((g4 ^ ((r16 >> 1) & 7)) << 4);
  const unsigned frag1 = frag0 ^ 64;
  LAS float* red = (LAS float*)(lds + RED_OFF);
  using T = std::true_type; using Fa = std::false_type;
  const __amdgpu_buffer_rsrc_t rsW = __builtin_amdgcn_make_buffer_rsrc(ws, 0, SLAB_OFF, 0x00020000);
  const i32x4 srdN = make_srd(w1, W1T_BYTES);

  const int p = wid - 4, r8 = lane >> 3;
  const i32x4 srdI = make_srd(idx, NB * NF * 4);
  const i32x4 srdT = make_srd(table, (unsigned)table_bytes);
  const unsigned chE = (unsigned)(((lane & 7) ^ (lane >> 4)) << 4);
  const unsigned chO = chE ^ 64;
  const unsigned ldsA = lds0 + p * 2048;
  int ia[4];
#define DA(J, I) { SB dma16((((unsigned)ia[I] << 7) + ((I & 1) ? chO : chE)), srdT, ldsA + ((J) & 3) * A_STAGE + (I >> 1) * 8192 + (I & 1) * 1024); SB }
#define DA4(J) DA(J, 0) DA(J, 1) DA(J, 2) DA(J, 3)
#define EA(J) ia[0] = e0[J]; ia[1] = e1[J]; ia[2] = e2[J]; ia[3] = e3[J];
  NSet raw; u32x2 ep; u32x4 bq0, bq1; unsigned w2v, b2v;
  i32x4 e0, e1, e2, e3;
  if (wid < 4) {
    N_LD8(raw, g4 * 4096 + wid * 128 + r16 * 8, (unsigned)(bid < 200 ? bid : 0) * 16384u)
    asm volatile("global_load_dwordx2 %0, %1, off sc1" : "=v"(ep) : "v"(ws + EPOCH_OFF + wid * 256) : "memory");
    asm volatile("global_load_dwordx4 %0, %1, off" : "=v"(bq0) : "v"(b1 + wid * 64 + g4 * 16) : "memory");
    asm volatile("global_load_dwordx4 %0, %1, off offset:16" : "=v"(bq1) : "v"(b1 + wid * 64 + g4 * 16) : "memory");
    asm volatile("global_load_ushort %0, %1, off" : "=v"(w2v) : "v"(w2 + wid * 64 + lane) : "memory");
    asm volatile("global_load_ushort %0, %1, off" : "=v"(b2v) : "v"(b2) : "memory");
  } else {
    const unsigned eq = (unsigned)(spar + 16 * p + r8) * 400u + FB * 4u * h, eo = (unsigned)(sown + 16 * p + r8) * 400u + FB * 4u * h;
    asm volatile("buffer_load_dwordx4 %0, %1, %2, 0 offen" : "=v"(e0) : "v"(eq), "s"(srdI) : "memory");
    asm volatile("buffer_load_dwordx4 %0, %1, %2, 0 offen offset:3200" : "=v"(e1) : "v"(eq), "s"(srdI) : "memory");
    asm volatile("buffer_load_dwordx4 %0, %1, %2, 0 offen" : "=v"(e2) : "v"(eo), "s"(srdI) : "memory");
    asm volatile("buffer_load_dwordx4 %0, %1, %2, 0 offen offset:3200" : "=v"(e3) : "v"(eo), "s"(srdI) : "memory");
    const i32x4 srdP = make_srd(wpg, NF * NF * 2);
#pragma unroll
    for (int i = 0; i < 5; ++i) dma16(p * 5120 + i * 1024 + lane * 16, srdP, lds0 + WPL_OFF + p * 5120 + i * 1024);
    asm volatile("s_waitcnt vmcnt(5)" : "+v"(e0), "+v"(e1), "+v"(e2), "+v"(e3) :: "memory");
    TL(1)
    EA(0) DA4(0) EA(1) DA4(1)
  }
  int role = wid;
  asm volatile("" : "+s"(role));
  if (role >= 4) {
    __builtin_amdgcn_s_setprio(PNN_PRIO);
    unsigned aLo = lds0 + IDXP_OFF + ((16 * p + r8) * QP - 2) * 4;
    unsigned aHi = lds0 + IDXO_OFF + ((16 * p + r8) * OP + FB * h) * 4;
    unsigned aP = lds0 + IDXO_OFF + (16 * p * OP + r8) * 4;
    auto ldi = [&](unsigned a) { return *(LAS const int*)(size_t)a; };
    const unsigned ldsP = lds0 + P_OFF + p * P_WAVE;
    auto swz = [&](int row) { return (unsigned)(row * 128 + ((g4 ^ ((row >> 1) & 7)) << 4)); };
    unsigned aF0 = ldsP + frag0, aF1 = ldsP + frag1;
    unsigned aD0 = ldsP + (r16 < 12 ? frag0 : 12288 + swz(r16 - 12)), aD1 = aD0 + (r16 < 12 ? 2048 : 0);
    unsigned aX1 = ldsP + (r16 < 8 ? (r16 >> 2) * 2048 + swz(12 + (r16 & 3)) : 12288 + swz(r16 - 8));
    unsigned aX2 = ldsP + (2 + (r16 >> 2)) * 2048 + swz(12 + (r16 & 3));
    unsigned aD0k = aD0 ^ 64, aD1k = aD1 ^ 64, aX1k = aX1 ^ 64, aX2k = aX2 ^ 64;
    asm volatile("" : "+v"(aF0), "+v"(aF1), "+v"(aD0), "+v"(aD1), "+v"(aX1), "+v"(aX2), "+v"(aD0k), "+v"(aD1k), "+v"(aX1k), "+v"(aX2k));
    auto ldf = [&](unsigned a) { return *(LAS const u32x4*)(size_t)a; };
    const unsigned tailmask = r8 < 4 ? 0u : 0x80000000u;

    int ip[5];
#define LDA(J) ia[0] = ldi(aLo + 4 * (J)); ia[1] = ldi(aLo + 4 * (J) + 32 * QP); ia[2] = ldi(aHi + 4 * (J)); ia[3] = ldi(aHi + 4 * (J) + 32 * OP);
#define LDP(S, G) _Pragma("unroll") for (int i = 0; i < 4; ++i) ip[i] = ldi(aP + 4 * OP * (S) + 128 * G + 32 * i); \
                  if constexpr (G == 0) ip[4] = ldi(aP + 4 * OP * (S) + 384);
#define DP(G, I) { SB dma16((((unsigned)ip[I] << 7) + ((I & 1) ? chO : chE)) | PKILL, srdT, ldsP + G * 4096 + I * 1024); SB }
#define DT { SB dma16((((unsigned)ip[4] << 7) + chE) | tailmask | PKILL, srdT, ldsP + 12288); SB }
#define DP4(G) DP(G, 0) DP(G, 1) DP(G, 2) DP(G, 3)
    auto stage_idx = [&]() {
      const unsigned oo = (unsigned)(sown + 16 * p) * 400u, op = (unsigned)(spar + 16 * p) * 400u + (FB * h + 2) * 4u;
#pragma unroll
      for (int i = 0; i < 3; ++i) {
        const unsigned c = lane + 64 * i, row = c / 12, k = c % 12;
        dma16(row * 400 + (k < 11 ? op + k * 16 : oo + (MAINS + 4) * 4), srdI, lds0 + IDXP_OFF + p * (16 * QP * 4) + i * 1024);
      }
#pragma unroll
      for (int i = 0; i < 7; ++i) {
        const unsigned c = lane + 64 * i, row = c / 26, k = c % 26;
        if (i < 6 || lane < 32) dma16(oo + row * 400 + (k < 25 ? k * 16 : MAINS * 4), srdI, lds0 + IDXO_OFF + p * (16 * OP * 4) + i * 1024);
      }
    };
    static_assert(MID == 8, "index tables");
    waitv<8>();
    TL(2)
    bar();
    const u32x2 tk = *(LAS const u32x2*)(lds + TOK_OFF + p * 8);
    const unsigned tlo = __builtin_amdgcn_readfirstlane(tk[0]), thi = __builtin_amdgcn_readfirstlane(tk[1]);
    f32x4 wp[23];
    {
      LAS const char* wl = lds + WPL_OFF;
      u32x4 R[6][4];
#pragma unroll
      for (int t = 0; t < 6; ++t)
#pragma unroll
        for (int kb = 0; kb < 4; ++kb) {
          LAS const char* rp = wl + (16 * t + r16) * 200 + 16 * g4 + 64 * kb;
          const u32x2 lo = *(LAS const u32x2*)rp, hi = *(LAS const u32x2*)(rp + 8);
          R[t][kb] = u32x4{lo[0], lo[1], hi[0], hi[1]};
        }
      auto one = [&](int k) {
        u32x4 v;
#pragma unroll
        for (int i = 0; i < 4; ++i) { const int d = k - 8 * g4 - 2 * i; v[i] = d == 0 ? 0x00003F80u : d == 1 ? 0x3F800000u : 0u; }
        return v;
      };
      auto w4 = [&](int row, int col0) {
        const u32x2 v = *(LAS const u32x2*)(wl + row * 200 + col0 * 2);
        return f32x4{__builtin_bit_cast(float, v[0] << 16), __builtin_bit_cast(float, v[0] & 0xffff0000u),
                     __builtin_bit_cast(float, v[1] << 16), __builtin_bit_cast(float, v[1] & 0xffff0000u)};
      };
      const f32x4 z = f32x4{0.f, 0.f, 0.f, 0.f};
      const u32x4 sel[2] = {one(r16), one(16 + r16)};
      const u32x4 selB[2] = {one(r16 < 12 ? r16 : -9), one(r16 < 12 ? 16 + r16 : -9)}, selT = one(r16 < 12 ? -9 : r16 - 12);
#pragma unroll
      for (int q = 1; q < 6; ++q)
#pragma unroll
        for (int u = 0; u < q; ++u) wp[q * (q - 1) / 2 + u] = MFMA(sel[u & 1], R[q][u >> 1], MFMA(R[u][q >> 1], sel[q & 1], z));
#pragma unroll
      for (int t = 0; t < 6; ++t) {
        const f32x4 d1 = MFMA(R[t][3], selT, MFMA(R[t][t >> 1], selB[t & 1], z));
        wp[15 + t] = w4(r16 < 12 ? 16 * t + r16 : 84 + r16, 16 * t + 4 * g4) + (g4 == 3 || r16 >= 12 ? d1 : z);
      }
      const bool same = g4 == (r16 >> 2);
      const int f1 = g4 < 2 ? 16 * g4 + 12 : 96, f2 = 16 * g4 + 44;
      wp[21] = same && g4 < 3 ? w4(f1 + (r16 & 3), f1) : z;
      wp[22] = same ? w4(f2 + (r16 & 3), f2) : z;
#pragma unroll
      for (int t = 0; t < 23; ++t) asm volatile("" : "+v"(wp[t]));
    }
    TL(3)
    waitv<0>();
    bar();
    TL(4)
    EA(2) DA4(2)
    EA(3) DA4(3) stage_idx();
    waitv<14>(); bar();
    waitv<0>(); LDA(4)
    static_assert(G0 >= 3, "start-up schedule");
#pragma unroll
    for (int n = 1; n < G0 - 2; ++n) { DA4(n + 3) LDA(n + 4) waitv<4>(); bar(); }
    LDP(0, 0) DA4(G0 + 1) DP4(0) DT LDA(G0 + 2) LDP(0, 1) waitv<9>();
    TL(5)
    bar();
    DA4(G0 + 2) DP4(1) LDA(G0 + 3) LDP(0, 2) waitv<8>(); bar();

    u32x4 fr[9][2];
#pragma unroll
    for (int t = 2; t < 6; ++t) { fr[t][0] = u32x4{0, 0, 0, 0}; fr[t][1] = u32x4{0, 0, 0, 0}; }
    float ps0 = 0.f, ps1 = 0.f, ps2 = 0.f, ps3 = 0.f;
    f32x4 gq[2][2];
    auto pm = [&](auto cc, auto u0c, auto q0c, auto u1c, auto q1c) {
      constexpr int c = decltype(cc)::value, u0 = decltype(u0c)::value, q0 = decltype(q0c)::value, u1 = decltype(u1c)::value, q1 = decltype(q1c)::value;
      const f32x4 z = f32x4{0.f, 0.f, 0.f, 0.f};
      SB
      __builtin_amdgcn_s_setprio(PM_PRIO);
      gq[c][0] = MFMA(fr[u0][0], fr[q0][0], z);
      if constexpr (u1 >= 0) gq[c][1] = MFMA(fr[u1][0], fr[q1][0], z);
      gq[c][0] = MFMA(fr[u0][1], fr[q0][1], gq[c][0]);
      if constexpr (u1 >= 0) gq[c][1] = MFMA(fr[u1][1], fr[q1][1], gq[c][1]);
      SB __builtin_amdgcn_s_setprio(PF_PRIO);
      SB
    };
    auto pf = [&](auto cc, auto w0c, auto w1c) {
      constexpr int c = decltype(cc)::value, i0 = decltype(w0c)::value, i1 = decltype(w1c)::value;
      const f32x4 w0 = wp[i0];
      ps0 = __builtin_fmaf(gq[c][0][0], w0[0], ps0); ps1 = __builtin_fmaf(gq[c][0][1], w0[1], ps1);
      ps2 = __builtin_fmaf(gq[c][0][2], w0[2], ps2); ps3 = __builtin_fmaf(gq[c][0][3], w0[3], ps3);
      if constexpr (i1 >= 0) {
        const f32x4 w1v = wp[i1];
        ps0 = __builtin_fmaf(gq[c][1][0], w1v[0], ps0); ps1 = __builtin_fmaf(gq[c][1][1], w1v[1], ps1);
        ps2 = __builtin_fmaf(gq[c][1][2], w1v[2], ps2); ps3 = __builtin_fmaf(gq[c][1][3], w1v[3], ps3);
      }
      SB
    };
#define PM(C, U0, Q0, U1, Q1) pm(IC(C), IC(U0), IC(Q0), IC(U1), IC(Q1));
#define PF(C, I0, I1) pf(IC(C), IC(I0), IC(I1));
#define WO(U, Q) (Q * (Q - 1) / 2 + U)
    float pv;
    auto total0 = [&]() {
      asm("" : "+v"(ps0)); asm("" : "+v"(ps1)); asm("" : "+v"(ps2)); asm("" : "+v"(ps3));
      pv = (ps0 + ps1) + (ps2 + ps3);
      asm volatile("" : "+v"(pv));
      ps0 = ps1 = ps2 = ps3 = 0.f;
    };
    auto total1 = [&](int s) {
      float tot = wave_sum63(pv);
      if (lane == 63 && s >= 0) red[256 + 16 * p + s] = tot;
    };
#define RDF(D, T) fr[D][0] = ldf(aF0 + T * 2048); fr[D][1] = ldf(aF1 + T * 2048);
#define RDX(D, A) fr[D][0] = ldf(A); fr[D][1] = ldf(A##k);
#define RDD(T) if (r16 < 12) { fr[6][0] = ldf(aD0 + T * 2048); fr[6][1] = ldf(aD0k + T * 2048); \
                              fr[7][0] = ldf(aD0 + (T + 1) * 2048); fr[7][1] = ldf(aD0k + (T + 1) * 2048); }
    auto xpoll = [&]() {
      GAS u64* pf = (GAS u64*)(ws + XFLAG_OFF + (bid ^ 8) * 256 + p * 64);
      const u64 tok = ((u64)thi << 32) | tlo;
      for (unsigned spins = 0; spins < (1u << 18); ++spins) {
        if (__all(__hip_atomic_load(pf, RLX_AGENT) == tok)) break;
        __builtin_amdgcn_s_sleep(1);
      }
      asm volatile("" ::: "memory");
    };
    auto steps = [&](int s, auto fac, auto nbarc, auto lastc) {
      constexpr bool fa = decltype(fac)::value, last = decltype(lastc)::value;
      constexpr int nbar = decltype(nbarc)::value;
      const int n = 3 * s + G0;
#define XA(J, I) if constexpr (fa) DA(J, I)
#define XP(G, I) if constexpr (!last) DP(G, I)
#define ENDSTEP(CNT, K) { float x0 = ps0, x1 = ps1, x2 = ps2, x3 = ps3; asm volatile("" : "+v"(x0), "+v"(x1), "+v"(x2), "+v"(x3)); ps0 = x0; ps1 = x1; ps2 = x2; ps3 = x3; } \
      STAMP(1) waitv<CNT>(); STAMP(2) if constexpr (nbar == 2 && K == 1) xpoll(); if constexpr (nbar > K) bar(); else { SB asm volatile("s_waitcnt lgkmcnt(0)" ::: "memory"); SB } STAMP(3)
      asm volatile("" : "+v"(aLo), "+v"(aHi), "+v"(aP));
      RDF(0, 0) RDF(1, 1) RDX(6, aD0) RDX(7, aD1) RDX(8, aX1)
      PM(0, 4, 5, 3, 5) XA(n + 3, 0) DP(2, 0)
      PM(1, 3, 4, 2, 5) PF(0, WO(4, 5), WO(3, 5)) XA(n + 3, 1) DP(2, 1)
      PM(0, 0, 1, 0, 6) PF(1, WO(3, 4), WO(2, 5)) total0(); if constexpr (!TOTB) total1(s - 1); XA(n + 3, 2) DP(2, 2)
      PM(1, 1, 7, 8, 8) PF(0, WO(0, 1), 15) XA(n + 3, 3) DP(2, 3) PF(1, 16, 21)
      if constexpr (fa) { LDA(G0 + 4) }
      if constexpr (!last) { LDP(1, 0) }
      ENDSTEP((fa ? 8 : 4), 0)
      RDF(2, 2) RDF(3, 3) RDD(2)
      XA(n + 4, 0) XP(0, 0)
      if constexpr (TOTB) total1(s - 1);
      PM(0, 0, 2, 1, 2) XA(n + 4, 1) XP(0, 1)
      PM(1, 0, 3, 1, 3) PF(0, WO(0, 2), WO(1, 2)) XA(n + 4, 2) XP(0, 2)
      PM(0, 2, 3, 2, 6) PF(1, WO(0, 3), WO(1, 3)) XA(n + 4, 3) XP(0, 3)
      PM(1, 3, 7, -1, -1) PF(0, WO(2, 3), 17)
      if constexpr (!last) DT
      PF(1, 18, -1)
      if constexpr (fa) { LDA(G0 + 5) }
      if constexpr (!last) { LDP(1, 1) }
      ENDSTEP(((fa ? 4 : 0) + (last ? 0 : 5)), 1)
      RDX(8, aX2) RDF(4, 4) RDF(5, 5) RDD(4)
      XA(n + 5, 0)
      PM(0, 8, 8, 0, 4) XP(1, 0) XA(n + 5, 1)
      PM(1, 1, 4, 2, 4) PF(0, 22, WO(0, 4)) XP(1, 1) XA(n + 5, 2)
      PM(0, 0, 5, 1, 5) PF(1, WO(1, 4), WO(2, 4)) XP(1, 2) XA(n + 5, 3)
      PM(1, 4, 6, 5, 7) PF(0, WO(0, 5), WO(1, 5)) XP(1, 3) PF(1, 19, 20)
      if constexpr (fa) { LDA(G0 + 6) }
      if constexpr (!last) { LDP(1, 2) }
      aLo += 12; aHi += 12; aP += 4 * OP;
      ENDSTEP(((fa ? 4 : 0) + (last ? 0 : 4)), 2)
    };
    for (int s = 0; s < S2 - 1; ++s) steps(s, T{}, IC(3), Fa{});
    if constexpr (POST == 0) steps(15, Fa{}, IC(2), T{});
    else {
      steps(S2 - 1, Fa{}, IC(2), Fa{});
      for (int s = S2; s < 15; ++s) steps(s, Fa{}, IC(0), Fa{});
      steps(15, Fa{}, IC(0), T{});
    }
    PM(0, 4, 5, 3, 5) PM(1, 3, 4, 2, 5) PF(0, WO(4, 5), WO(3, 5)) PF(1, WO(3, 4), WO(2, 5))
    total0(); total1(15);
    if (bid == 0) {
      sweep2(rsW, WFLAG_OFF + p * 2048 + lane * 16, tlo, thi);
      sweep2(rsW, WFLAG_OFF + p * 2048 + 1024 + lane * 16, tlo, thi);
      if (lane == 0) __hip_atomic_store((GAS u64*)(ws + EPOCH_OFF + p * 256), ((u64)thi << 32) | tlo, RLX_AGENT);
    }
  } else {
    LAS float* bw = (LAS float*)(lds + BW_OFF);
    const i32x4 srdW = make_srd(ws, W1T_BYTES);
    const unsigned voffW = lane * 16;
    const unsigned wbase = h * (FB * 32768) + wid * 8192;
    WSet w0, w1s, w2s, w3;
#define W_LD4(S, OFF) w_ld<0>(S, voffW, srdW, OFF); w_ld<1>(S, voffW, srdW, OFF); w_ld<2>(S, voffW, srdW, OFF); w_ld<3>(S, voffW, srdW, OFF);
    f32x4 acc[8][4];
    u64 tok;
    {
      n_wait<4>(raw);
      asm volatile("" : "+v"(ep));
      TL(1)
      WSet o; n_frag(raw, o);
      const unsigned dst = bid < 200 ? (bid >> 1) * 32768 + wid * 8192 + (bid & 1) * 4096 + lane * 16 : 0x80000000u;
#pragma unroll
      for (int c = 0; c < 4; ++c) __builtin_amdgcn_raw_buffer_store_b128(o.r[c], rsW, dst + c * 1024, 0, 16);
      const u64 e = ((u64)(unsigned)__builtin_amdgcn_readfirstlane((int)ep[1]) << 32) | (unsigned)__builtin_amdgcn_readfirstlane((int)ep[0]);
      tok = (e ^ 0x9E3779B97F4A7C15ull) * 0xBF58476D1CE4E5B9ull + 0x632BE59BD9B4E019ull;
      if (tok == e) tok = ~e;
      asm volatile("s_waitcnt vmcnt(0)" : "+v"(bq0), "+v"(bq1), "+v"(w2v), "+v"(b2v) :: "memory");
      TL(2)
      if (lane == 0) {
        __hip_atomic_store((GAS u64*)(ws + WFLAG_OFF + wid * 2048 + bid * 8), tok, RLX_AGENT);
        *(LAS u64*)(lds + TOK_OFF + wid * 8) = tok;
      }
      bw[wid * 64 + lane] = __builtin_bit_cast(float, w2v << 16);
      if (tid == 0) bw[256] = __builtin_bit_cast(float, b2v << 16);
#pragma unroll
      for (int m = 0; m < 8; ++m)
#pragma unroll
        for (int nn = 0; nn < 4; ++nn) {
#pragma unroll
          for (int r = 0; r < 4; ++r) {
            const unsigned d = (r < 2 ? bq0 : bq1)[(r & 1) * 2 + (nn >> 1)];
            acc[m][nn][r] = m < 4 ? 0.f : __builtin_bit_cast(float, (nn & 1) ? (d & 0xffff0000u) : (d << 16));
          }
          asm volatile("" : "+v"(acc[m][nn]));
        }
      bar();
      waitv<0>();
      __builtin_amdgcn_s_sleep(PDELAY);
      TL(3)
      sweep2(rsW, WFLAG_OFF + wid * 2048 + h * (2 * MAINS * 8) + (lane < FB ? lane : FB - 1) * 16, (unsigned)tok, (unsigned)(tok >> 32));
      TL(4)
    }
    W_LD4(w0, wbase) W_LD4(w1s, wbase + 4096) W_LD4(w2s, wbase + 32768)
    TL(5)
    bar();
    TL(6)
    u32x4 a[4];
    unsigned b0 = frag0, b1a = frag1;
#pragma unroll
    for (int m = 0; m < 3; ++m) a[m] = *(LAS u32x4*)(lds + b0 + m * 2048);
#define GRP(CUR, M, RD, LD) \
      mfma1(acc[M][0], CUR.r[0], a[M & 3]); SB RD SB \
      mfma1(acc[M][1], CUR.r[1], a[M & 3]); LD \
      mfma1(acc[M][2], CUR.r[2], a[M & 3]); touch(a[(M + 1) & 3]); \
      mfma1(acc[M][3], CUR.r[3], a[M & 3]); SB
#define RDK(M) if constexpr (M < 5) a[(M + 3) & 3] = *(LAS u32x4*)(lds + cb + co + (M + 3) * 2048); \
               else a[(M + 3) & 3] = *(LAS u32x4*)(lds + nb + no + (M - 5) * 2048);
    auto kstep = [&](WSet& cur, WSet& nxt, unsigned soff, unsigned cb, auto coc, unsigned nb, auto noc) {
      constexpr int co = decltype(coc)::value, no = decltype(noc)::value;
      w_wait<8>(cur);
      SB
#define G(M) GRP(cur, M, RDK(M), if constexpr ((M & 1) == 0) w_ld<M / 2>(nxt, voffW, srdW, soff);)
      G(0) G(1) G(2) G(3) G(4) G(5) G(6) G(7)
#undef G
    };
    unsigned so = wbase + 32768 + 4096;
    const unsigned wmid = MAINS * 32768 + wid * 8192;
    for (int n = 0; n < MAINS; n += 2) {
      kstep(w0, w3, so, b0, IC(0), b1a, IC(0));
      if (n == MAINS - 2) so = wmid + t_woff(0) - 28672;
      kstep(w1s, w0, so + 28672, b1a, IC(0), b0, IC(A_STAGE));
      STAMP(0) bar_nw(); STAMP(3)
      kstep(w2s, w1s, so + 32768, b0, IC(A_STAGE), b1a, IC(A_STAGE));
      b0 ^= 2 * A_STAGE;
      if (n == MAINS - 2) so = wmid + t_woff(2) - 32768 - 28672;
      kstep(w3, w2s, so + 32768 + 28672, b1a, IC(A_STAGE), b0, IC(0));
      b1a ^= 2 * A_STAGE;
      STAMP(0) bar_nw(); STAMP(3)
      so += 65536;
    }
    u32x4 px[8];
    {
      const i32x4 srdS = make_srd(ws + SLAB_OFF + (size_t)bid * SLAB_BYTES, SLAB_BYTES);
      const i32x4 srdR = make_srd(ws + SLAB_OFF + (size_t)(bid ^ 8) * SLAB_BYTES, SLAB_BYTES);
      WSet ts[NU];
      ts[0] = w0; ts[1] = w1s; ts[2] = w2s;
      auto top = [&](auto uc, auto gc) {
        constexpr int u = decltype(uc)::value, g = decltype(gc)::value, st = t_store(u, g), ld = t_load(u, g), x = t_w0(u) + t_wn(u) * g;
        if constexpr (st >= 0) {
          const f32x4 lo = acc[st >> 1][2 * (st & 1)], hi = acc[st >> 1][2 * (st & 1) + 1];
          x_st<st & 3>(u32x4{pk_bf16(lo[0], lo[1]), pk_bf16(lo[2], lo[3]), pk_bf16(hi[0], hi[1]), pk_bf16(hi[2], hi[3])}, voffW, srdS, wid * 8192 + (st >> 2) * 4096);
        }
        if constexpr (ld >= 0) x_ld<ld & 3>(px[ld], voffW, srdR, wid * 8192 + (ld >> 2) * 4096);
        if constexpr (t_wn(u) > 0) w_ld<x & 3>(ts[x >> 2], voffW, srdW, wmid + t_woff(x >> 2));
        if constexpr (t_wn(u) > 1) w_ld<(x + 1) & 3>(ts[x >> 2], voffW, srdW, wmid + t_woff(x >> 2));
      };
      auto hk = [&](auto uc) {
        constexpr int U = decltype(uc)::value;
        constexpr int co = ((MAINS + U / 4) & 3) * A_STAGE + ((U >> 1) & 1) * 8192;
        constexpr int no = ((MAINS + (U + 1) / 4) & 3) * A_STAGE + (((U + 1) >> 1) & 1) * 8192;
        const unsigned cb = (U & 1) ? frag1 : frag0, nb = (U & 1) ? frag0 : frag1;
        WSet& cur = ts[U];
        w_wait<tail_wait(U)>(cur);
        if constexpr (U == FLAG_U) {
          if (lane == 0) __hip_atomic_store((GAS u64*)(ws + XFLAG_OFF + bid * 256 + wid * 64), tok, RLX_AGENT);
        }
        SB
#define RDR(M) if constexpr (M == 0) a[3] = *(LAS u32x4*)(lds + cb + co + 3 * 2048); \
               else if constexpr (U + 1 < NU) a[M - 1] = *(LAS u32x4*)(lds + nb + no + (M - 1) * 2048);
        GRP(cur, 4, RDR(0), top(uc, IC(0));) GRP(cur, 5, RDR(1), top(uc, IC(1));) GRP(cur, 6, RDR(2), top(uc, IC(2));) GRP(cur, 7, RDR(3), top(uc, IC(3));)
      };
#define HK4(J) hk(IC(4 * J)); hk(IC(4 * J + 1)); hk(IC(4 * J + 2)); hk(IC(4 * J + 3));
      HK4(0) bar_nw(); HK4(1) bar_nw(); HK4(2) bar_nw();
      HK4((MID / 2 - 1))
      asm volatile("s_nop 7"); SB
    }
    __builtin_amdgcn_s_setprio(2);
    f32x4 ww[4];
#pragma unroll
    for (int r = 0; r < 4; ++r) ww[r] = *(LAS f32x4*)(bw + wid * 64 + g4 * 16 + r * 4);
    f32x4 wt[4];
#pragma unroll
    for (int nn = 0; nn < 4; ++nn) wt[nn] = f32x4{ww[0][nn], ww[1][nn], ww[2][nn], ww[3][nn]};
#pragma unroll
    for (int m = 0; m < 4; ++m) {
      float s = 0.f;
      if (m == 0) x_wait<6>(px[0], px[1]); else if (m == 1) x_wait<4>(px[2], px[3]); else if (m == 2) x_wait<2>(px[4], px[5]); else x_wait<0>(px[6], px[7]);
      f32x2 s2 = f32x2{0.f, 0.f};
#pragma unroll
      for (int nn = 0; nn < 4; ++nn)
#pragma unroll
        for (int r = 0; r < 4; r += 2) {
          const unsigned pd = px[2 * m + (nn >> 1)][2 * (nn & 1) + (r >> 1)];
          const f32x2 x = f32x2{acc[4 + m][nn][r], acc[4 + m][nn][r + 1]} + f32x2{__builtin_bit_cast(float, pd << 16), __builtin_bit_cast(float, pd & 0xffff0000u)};
          const f32x2 t = __builtin_elementwise_fma(x * x, f32x2{-0.1029432397f, -0.1029432397f}, f32x2{-2.302208198f, -2.302208198f});
          const f32x2 z = x * t;
          const f32x2 d = f32x2{__builtin_amdgcn_exp2f(z[0]), __builtin_amdgcn_exp2f(z[1])} + f32x2{1.f, 1.f};
          const f32x2 y = x * f32x2{wt[nn][r], wt[nn][r + 1]};
          s2 = __builtin_elementwise_fma(y, f32x2{__builtin_amdgcn_rcpf(d[0]), __builtin_amdgcn_rcpf(d[1])}, s2);
        }
      s = s2[0] + s2[1];
      s += __shfl_xor(s, 16);
      s += __shfl_xor(s, 32);
      if (lane < 16) red[wid * 64 + m * 16 + lane] = s;
    }
  }
  bar();
  if (tid < 64) {
    float v = red[256 + tid] + ((red[tid] + red[64 + tid]) + (red[128 + tid] + red[192 + tid])) + *(LAS const float*)(lds + BW_OFF + 1024);
    out[sown + tid] = __float2bfloat16(v);
  }
}

extern "C" void kernel_launch(void* const* d_in, const int* in_sizes, int n_in, void* d_out, int out_size, void* d_ws, size_t ws_size,
                              hipStream_t stream) {
  (void)hipFuncSetAttribute((const void*)pnn_kernel, hipFuncAttributeMaxDynamicSharedMemorySize, LDS_BYTES);
  pnn_kernel<<<NWG, 512, LDS_BYTES, stream>>>((const int*)d_in[0], (const char*)d_in[1], (u64)in_sizes[1] * 2ull, (const char*)d_in[2],
                                              (const char*)d_in[3], (const unsigned short*)d_in[4], (const unsigned short*)d_in[5],
                                              (const unsigned short*)d_in[6], (char*)d_ws, (__hip_bfloat16*)d_out);
}
```

```cpp
#define DIAG
#include <hip/hip_runtime.h>
#include <hip/hip_bf16.h>
#include <stdint.h>
#include <stdio.h>
#include <type_traits>

typedef float f32x4 __attribute__((ext_vector_type(4)));
typedef float f32x2 __attribute__((ext_vector_type(2)));
typedef int i32x4 __attribute__((ext_vector_type(4)));
typedef int i32x2 __attribute__((ext_vector_type(2)));
typedef unsigned u32x4 __attribute__((ext_vector_type(4)));
typedef unsigned u32x2 __attribute__((ext_vector_type(2)));
typedef __bf16 bf16x8 __attribute__((ext_vector_type(8)));
typedef unsigned long long u64;
#define LAS __attribute__((address_space(3)))
#define GAS __attribute__((address_space(1)))

constexpr int NB = 16384, NF = 100, ND = 64, NH = 256, HF = 50;
constexpr int NWG = 256;
#define POST 1
#define PHOLD 0
#define PKG 1
#define PSLEEP 8
#define PDELAY 12
#define HIDEF 0
#define TOTB 1
#define STG 3
#define PKILL 0u
#define PM_PRIO 1
#define PF_PRIO 0
#define PNN_PRIO 1
#define MID 8
constexpr int MAINS = (NF - MID) / 2, FB = MAINS + MID, NU = 2 * MID;
constexpr int S2 = 16 - POST;
constexpr int G0 = HF - 3 * S2;
static_assert(G0 >= 2 && MAINS % 2 == 0 && MAINS <= 48, "schedule");
constexpr int FLAG_U = NU - 8;
constexpr int t_store(int u, int g) {
  if (MID >= 12) return g == 1 && u < 8 ? u : -1;
  return u < 3 ? ((g & 1) ? 2 * u + (g >> 1) : -1) : (u < 5 && g == 1 ? u + 3 : -1);
}
constexpr int t_load(int u, int g) { return u >= NU - 4 && (g & 1) ? 2 * (u - NU + 4) + (g >> 1) : -1; }
constexpr int t_iss(int v) {
  if (v < 3) return -1;
  if (v <= FLAG_U + 3) { const int x = FLAG_U - 2 - (FLAG_U + 3 - v) / 2; return v - 3 < x ? v - 3 : x; }
  return FLAG_U + (v - FLAG_U - 4) / 2;
}
constexpr int t_woff(int v) { return ((v >> 2) + ((v & 2) ? 0 : 4)) * 32768 + (v & 1) * 4096; }
constexpr int t_wn(int u) { int c = 0; for (int v = 3; v < NU; ++v) c += t_iss(v) == u; return c; }
constexpr int t_w0(int u) { for (int v = 3; v < NU; ++v) if (t_iss(v) == u) return 4 * v; return 0; }
constexpr int tail_wait(int u) {
  if (u == FLAG_U) return 0;
  int cnt = u < 3 ? 4 * (2 - u) : 0;
  for (int v = 0; v < u; ++v) {
    if (v == FLAG_U) ++cnt;
    for (int g = 0; g < 4; ++g) {
      cnt += (t_store(v, g) >= 0) + (t_load(v, g) >= 0);
      if (t_wn(v) > 0 && t_w0(v) + t_wn(v) * (g + 1) - 1 == 4 * u + 3) cnt = 0; else cnt += t_wn(v);
    }
  }
  return cnt;
}
constexpr int W1T_BYTES = NF * ND * NH * 2;
constexpr int EPOCH_OFF = W1T_BYTES;
constexpr int WFLAG_OFF = EPOCH_OFF + 1024;
constexpr int XFLAG_OFF = WFLAG_OFF + 8192;
constexpr int SLAB_OFF = XFLAG_OFF + NWG * 256;
constexpr int SLAB_BYTES = 32768;
constexpr int A_STAGE = 16384;
constexpr int P_OFF = 4 * A_STAGE;
constexpr int P_WAVE = 13 * 1024;
constexpr int WPL_OFF = 2 * A_STAGE;
constexpr int OP = 104;
constexpr int IDXO_OFF = P_OFF + 4 * P_WAVE;
constexpr int IDXP_OFF = IDXO_OFF + 64 * OP * 4;
constexpr int QP = 48;
constexpr int RED_OFF = IDXP_OFF + 64 * QP * 4;
constexpr int BW_OFF = RED_OFF + 5 * 64 * 4;
constexpr int TOK_OFF = BW_OFF + 260 * 4;
constexpr int LDS_BYTES = TOK_OFF + 32;
static_assert(LDS_BYTES <= 160 * 1024, "LDS");

__device__ __forceinline__ float bf2f(unsigned short h) { return __builtin_bit_cast(float, (unsigned)h << 16); }
__device__ __forceinline__ void dma16(unsigned voff, i32x4 srd, unsigned lds_dst) {
  asm volatile("s_mov_b32 m0, %0\n\ts_nop 0\n\tbuffer_load_dwordx4 %1, %2, 0 offen lds" :: "s"(lds_dst), "v"(voff), "s"(srd) : "memory");
}
template <int N> __device__ __forceinline__ void waitv() { asm volatile("s_waitcnt vmcnt(%0)" :: "i"(N) : "memory"); }
#define SB __builtin_amdgcn_sched_barrier(0);
__device__ __forceinline__ void bar() {
  SB asm volatile("s_waitcnt lgkmcnt(0)" ::: "memory");
  __builtin_amdgcn_s_barrier();
  asm volatile("" ::: "memory"); SB
}
__device__ __forceinline__ void bar_nw() {
  SB __builtin_amdgcn_s_barrier();
  asm volatile("" ::: "memory"); SB
}
__device__ __forceinline__ i32x4 make_srd(const void* p, unsigned bytes) {
  u64 a = (u64)p;
  i32x4 r;
  r[0] = __builtin_amdgcn_readfirstlane((int)(unsigned)a);
  r[1] = __builtin_amdgcn_readfirstlane((int)(unsigned)(a >> 32) & 0xffff);
  r[2] = (int)bytes;
  r[3] = 0x00020000;
  return r;
}
#define MFMA(a, b, c) __builtin_amdgcn_mfma_f32_16x16x32_bf16(__builtin_bit_cast(bf16x8, a), __builtin_bit_cast(bf16x8, b), c, 0, 0, 0)
#define IC(x) std::integral_constant<int, x>{}
#define RLX_AGENT __ATOMIC_RELAXED, __HIP_MEMORY_SCOPE_AGENT

struct WSet { u32x4 r[4]; };
template <int J> __device__ __forceinline__ void w_ld(WSet& s, unsigned voff, i32x4 srd, unsigned soff) {
  asm volatile("buffer_load_dwordx4 %0, %1, %2, %3 offen offset:%4 sc1" : "=v"(s.r[J]) : "v"(voff), "s"(srd), "s"(soff), "i"(J * 1024) : "memory");
}
template <int N> __device__ __forceinline__ void w_wait(WSet& s) {
  asm volatile("s_waitcnt vmcnt(%4)" : "+v"(s.r[0]), "+v"(s.r[1]), "+v"(s.r[2]), "+v"(s.r[3]) : "i"(N) : "memory");
}
template <int J> __device__ __forceinline__ void x_st(const u32x4& v, unsigned voff, i32x4 srd, unsigned soff) {
  asm volatile("buffer_store_dwordx4 %0, %1, %2, %3 offen offset:%4 sc1\n\ts_nop 1" :: "v"(v), "v"(voff), "s"(srd), "s"(soff), "i"(J * 1024) : "memory");
}
template <int J> __device__ __forceinline__ void x_ld(u32x4& v, unsigned voff, i32x4 srd, unsigned soff) {
  asm volatile("buffer_load_dwordx4 %0, %1, %2, %3 offen offset:%4 sc1" : "=v"(v) : "v"(voff), "s"(srd), "s"(soff), "i"(J * 1024) : "memory");
}
template <int N> __device__ __forceinline__ void x_wait(u32x4& c0, u32x4& c1) {
  asm volatile("s_waitcnt vmcnt(%2)" : "+v"(c0), "+v"(c1) : "i"(N) : "memory");
}
__device__ __forceinline__ unsigned pk_bf16(float lo, float hi) {
  typedef __bf16 bf16x2 __attribute__((ext_vector_type(2)));
  return __builtin_bit_cast(unsigned, bf16x2{(__bf16)lo, (__bf16)hi});
}
__device__ __forceinline__ void mfma1(f32x4& c, const u32x4& w, const u32x4& e) {
  asm volatile("v_mfma_f32_16x16x32_bf16 %0, %1, %2, %0" : "+v"(c) : "v"(w), "v"(e));
}
__device__ __forceinline__ void touch(const u32x4& x) { asm volatile("" :: "v"(x)); }
struct NSet { u32x2 r[8]; };
template <int J> __device__ __forceinline__ void n_ld(NSet& s, unsigned voff, i32x4 srd, unsigned soff) {
  asm volatile("buffer_load_dwordx2 %0, %1, %2, %3 offen offset:%4" : "=v"(s.r[J]) : "v"(voff), "s"(srd), "s"(soff), "i"(J * 512) : "memory");
}
template <int N> __device__ __forceinline__ void n_wait(NSet& s) {
  asm volatile("s_waitcnt vmcnt(%8)"
               : "+v"(s.r[0]), "+v"(s.r[1]), "+v"(s.r[2]), "+v"(s.r[3]), "+v"(s.r[4]), "+v"(s.r[5]), "+v"(s.r[6]), "+v"(s.r[7]) : "i"(N) : "memory");
}
__device__ __forceinline__ void n_frag(const NSet& s, WSet& o) {
#pragma unroll
  for (int c = 0; c < 4; ++c)
#pragma unroll
    for (int i = 0; i < 4; ++i) o.r[c][i] = __builtin_amdgcn_perm(s.r[2 * i + 1][c >> 1], s.r[2 * i][c >> 1], (c & 1) ? 0x07060302u : 0x05040100u);
}
#define N_LD8(S, V, OFF) n_ld<0>(S, V, srdN, OFF); n_ld<1>(S, V, srdN, OFF); n_ld<2>(S, V, srdN, OFF); n_ld<3>(S, V, srdN, OFF); \
                         n_ld<4>(S, V, srdN, OFF); n_ld<5>(S, V, srdN, OFF); n_ld<6>(S, V, srdN, OFF); n_ld<7>(S, V, srdN, OFF);
template <int CTRL, int RM> __device__ __forceinline__ float dpp_add(float v) {
  return v + __builtin_bit_cast(float, __builtin_amdgcn_update_dpp(0, __builtin_bit_cast(int, v), CTRL, RM, 0xf, true));
}
__device__ __forceinline__ float wave_sum63(float v) {
  v = dpp_add<0x111, 0xf>(v); v = dpp_add<0x112, 0xf>(v); v = dpp_add<0x114, 0xf>(v); v = dpp_add<0x118, 0xf>(v);
  v = dpp_add<0x142, 0xa>(v); v = dpp_add<0x143, 0xc>(v);
  return v;
}
__device__ __forceinline__ void sweep2(__amdgpu_buffer_rsrc_t rs, unsigned off, unsigned tlo, unsigned thi) {
  for (unsigned spins = 0; spins < (1u << 18); ++spins) {
    asm volatile("" ::: "memory");
    const u32x4 v = __builtin_amdgcn_raw_buffer_load_b128(rs, off, 0, 16);
    if (__all(v[0] == tlo && v[1] == thi && v[2] == tlo && v[3] == thi)) break;
    __builtin_amdgcn_s_sleep(PSLEEP);
  }
  asm volatile("" ::: "memory");
}
#define TL(i)
#define STAMP(i)

__global__ __launch_bounds__(512, 2) void pnn_kernel(const int* __restrict__ idx, const char* __restrict__ table, u64 table_bytes,
                                                     const char* __restrict__ wpg, const char* __restrict__ w1,
                                                     const unsigned short* __restrict__ b1, const unsigned short* __restrict__ w2,
                                                     const unsigned short* __restrict__ b2, char* __restrict__ ws,
                                                     __hip_bfloat16* __restrict__ out) {
  extern __shared__ __attribute__((aligned(16))) char lds_raw[];
  LAS char* lds = (LAS char*)lds_raw;
  const int tid = threadIdx.x, lane = tid & 63, wid = __builtin_amdgcn_readfirstlane(tid >> 6);
  const int bid = blockIdx.x, h = (bid >> 3) & 1;
  const int pair = (bid & 7) * 16 + (bid >> 4);
  const int sown = pair * 128 + 64 * h, spar = pair * 128 + 64 * (h ^ 1);
  u64 dg_c0 = __builtin_amdgcn_s_memtime(), dg_r0 = __builtin_amdgcn_s_memrealtime(), dg_r1 = 0, dg_r2 = 0, dg_r3 = 0, dg_r4 = 0;
  __builtin_amdgcn_s_waitcnt(0xC07F);
  const unsigned lds0 = (unsigned)(size_t)lds;
  const int r16 = lane & 15, g4 = lane >> 4;
  const unsigned frag0 = r16 * 128 + ((g4 ^ ((r16 >> 1) & 7)) << 4);
  const unsigned frag1 = frag0 ^ 64;
  LAS float* red = (LAS float*)(lds + RED_OFF);
  using T = std::true_type; using Fa = std::false_type;
  const __amdgpu_buffer_rsrc_t rsW = __builtin_amdgcn_make_buffer_rsrc(ws, 0, SLAB_OFF, 0x00020000);
  const i32x4 srdN = make_srd(w1, W1T_BYTES);

  const int p = wid - 4, r8 = lane >> 3;
  const i32x4 srdI = make_srd(idx, NB * NF * 4);
  const i32x4 srdT = make_srd(table, (unsigned)table_bytes);
  const unsigned chE = (unsigned)(((lane & 7) ^ (lane >> 4)) << 4);
  const unsigned chO = chE ^ 64;
  const unsigned ldsA = lds0 + p * 2048;
  int ia[4];
#define DA(J, I) { SB dma16((((unsigned)ia[I] << 7) + ((I & 1) ? chO : chE)), srdT, ldsA + ((J) & 3) * A_STAGE + (I >> 1) * 8192 + (I & 1) * 1024); SB }
#define DA4(J) DA(J, 0) DA(J, 1) DA(J, 2) DA(J, 3)
#define EA(J) ia[0] = e0[J]; ia[1] = e1[J]; ia[2] = e2[J]; ia[3] = e3[J];
  auto stage_idx = [&]() {
    const unsigned oo = (unsigned)(sown + 16 * p) * 400u, op = (unsigned)(spar + 16 * p) * 400u + (FB * h + 2) * 4u;
#pragma unroll
    for (int i = 0; i < 3; ++i) {
      const unsigned c = lane + 64 * i, row = c / 12, k = c % 12;
      dma16(row * 400 + (k < 11 ? op + k * 16 : oo + (MAINS + 4) * 4), srdI, lds0 + IDXP_OFF + p * (16 * QP * 4) + i * 1024);
    }
#pragma unroll
    for (int i = 0; i < 7; ++i) {
      const unsigned c = lane + 64 * i, row = c / 26, k = c % 26;
      if (i < 6 || lane < 32) dma16(oo + row * 400 + (k < 25 ? k * 16 : MAINS * 4), srdI, lds0 + IDXO_OFF + p * (16 * OP * 4) + i * 1024);
    }
  };
  NSet raw; u32x2 ep; u32x4 bq0, bq1; unsigned w2v, b2v;
  i32x4 e0, e1, e2, e3;
  if (wid < 4) {
    N_LD8(raw, g4 * 4096 + wid * 128 + r16 * 8, (unsigned)(bid < 200 ? bid : 0) * 16384u)
    asm volatile("global_load_dwordx2 %0, %1, off sc1" : "=v"(ep) : "v"(ws + EPOCH_OFF + wid * 256) : "memory");
    asm volatile("global_load_dwordx4 %0, %1, off" : "=v"(bq0) : "v"(b1 + wid * 64 + g4 * 16) : "memory");
    asm volatile("global_load_dwordx4 %0, %1, off offset:16" : "=v"(bq1) : "v"(b1 + wid * 64 + g4 * 16) : "memory");
    asm volatile("global_load_ushort %0, %1, off" : "=v"(w2v) : "v"(w2 + wid * 64 + lane) : "memory");
    asm volatile("global_load_ushort %0, %1, off" : "=v"(b2v) : "v"(b2) : "memory");
  } else {
    const unsigned eq = (unsigned)(spar + 16 * p + r8) * 400u + FB * 4u * h, eo = (unsigned)(sown + 16 * p + r8) * 400u + FB * 4u * h;
    asm volatile("buffer_load_dwordx4 %0, %1, %2, 0 offen" : "=v"(e0) : "v"(eq), "s"(srdI) : "memory");
    asm volatile("buffer_load_dwordx4 %0, %1, %2, 0 offen offset:3200" : "=v"(e1) : "v"(eq), "s"(srdI) : "memory");
    asm volatile("buffer_load_dwordx4 %0, %1, %2, 0 offen" : "=v"(e2) : "v"(eo), "s"(srdI) : "memory");
    asm volatile("buffer_load_dwordx4 %0, %1, %2, 0 offen offset:3200" : "=v"(e3) : "v"(eo), "s"(srdI) : "memory");
    const i32x4 srdP = make_srd(wpg, NF * NF * 2);
#pragma unroll
    for (int i = 0; i < 5; ++i) dma16(p * 5120 + i * 1024 + lane * 16, srdP, lds0 + WPL_OFF + p * 5120 + i * 1024);
    asm volatile("s_waitcnt vmcnt(5)" : "+v"(e0), "+v"(e1), "+v"(e2), "+v"(e3) :: "memory");
    TL(1)
    EA(0) DA4(0) EA(1) DA4(1)
  }
  int role = wid;
  asm volatile("" : "+s"(role));
  if (role >= 4) {
    __builtin_amdgcn_s_setprio(PNN_PRIO);
    unsigned aLo = lds0 + IDXP_OFF + ((16 * p + r8) * QP - 2) * 4;
    unsigned aHi = lds0 + IDXO_OFF + ((16 * p + r8) * OP + FB * h) * 4;
    unsigned aP = lds0 + IDXO_OFF + (16 * p * OP + r8) * 4;
    auto ldi = [&](unsigned a) { return *(LAS const int*)(size_t)a; };
    const unsigned ldsP = lds0 + P_OFF + p * P_WAVE;
    auto swz = [&](int row) { return (unsigned)(row * 128 + ((g4 ^ ((row >> 1) & 7)) << 4)); };
    unsigned aF0 = ldsP + frag0, aF1 = ldsP + frag1;
    unsigned aD0 = ldsP + (r16 < 12 ? frag0 : 12288 + swz(r16 - 12)), aD1 = aD0 + (r16 < 12 ? 2048 : 0);
    unsigned aX1 = ldsP + (r16 < 8 ? (r16 >> 2) * 2048 + swz(12 + (r16 & 3)) : 12288 + swz(r16 - 8));
    unsigned aX2 = ldsP + (2 + (r16 >> 2)) * 2048 + swz(12 + (r16 & 3));
    unsigned aD0k = aD0 ^ 64, aD1k = aD1 ^ 64, aX1k = aX1 ^ 64, aX2k = aX2 ^ 64;
    asm volatile("" : "+v"(aF0), "+v"(aF1), "+v"(aD0), "+v"(aD1), "+v"(aX1), "+v"(aX2), "+v"(aD0k), "+v"(aD1k), "+v"(aX1k), "+v"(aX2k));
    auto ldf = [&](unsigned a) { return *(LAS const u32x4*)(size_t)a; };
    const unsigned tailmask = r8 < 4 ? 0u : 0x80000000u;

    int ip[5];
#define LDA(J) ia[0] = ldi(aLo + 4 * (J)); ia[1] = ldi(aLo + 4 * (J) + 32 * QP); ia[2] = ldi(aHi + 4 * (J)); ia[3] = ldi(aHi + 4 * (J) + 32 * OP);
#define LDP(S, G) _Pragma("unroll") for (int i = 0; i < 4; ++i) ip[i] = ldi(aP + 4 * OP * (S) + 128 * G + 32 * i); \
                  if constexpr (G == 0) ip[4] = ldi(aP + 4 * OP * (S) + 384);
#define DP(G, I) { SB dma16((((unsigned)ip[I] << 7) + ((I & 1) ? chO : chE)) | PKILL, srdT, ldsP + G * 4096 + I * 1024); SB }
#define DT { SB dma16((((unsigned)ip[4] << 7) + chE) | tailmask | PKILL, srdT, ldsP + 12288); SB }
#define DP4(G) DP(G, 0) DP(G, 1) DP(G, 2) DP(G, 3)
    static_assert(MID == 8, "index tables");
    waitv<8>();
    TL(2)
    bar();
    const u32x2 tk = *(LAS const u32x2*)(lds + TOK_OFF + p * 8);
    const unsigned tlo = __builtin_amdgcn_readfirstlane(tk[0]), thi = __builtin_amdgcn_readfirstlane(tk[1]);
    f32x4 wp[23];
    {
      LAS const char* wl = lds + WPL_OFF;
      u32x4 R[6][4];
#pragma unroll
      for (int t = 0; t < 6; ++t)
#pragma unroll
        for (int kb = 0; kb < 4; ++kb) {
          LAS const char* rp = wl + (16 * t + r16) * 200 + 16 * g4 + 64 * kb;
          const u32x2 lo = *(LAS const u32x2*)rp, hi = *(LAS const u32x2*)(rp + 8);
          R[t][kb] = u32x4{lo[0], lo[1], hi[0], hi[1]};
        }
      auto one = [&](int k) {
        u32x4 v;
#pragma unroll
        for (int i = 0; i < 4; ++i) { const int d = k - 8 * g4 - 2 * i; v[i] = d == 0 ? 0x00003F80u : d == 1 ? 0x3F800000u : 0u; }
        return v;
      };
      auto w4 = [&](int row, int col0) {
        const u32x2 v = *(LAS const u32x2*)(wl + row * 200 + col0 * 2);
        return f32x4{__builtin_bit_cast(float, v[0] << 16), __builtin_bit_cast(float, v[0] & 0xffff0000u),
                     __builtin_bit_cast(float, v[1] << 16), __builtin_bit_cast(float, v[1] & 0xffff0000u)};
      };
      const f32x4 z = f32x4{0.f, 0.f, 0.f, 0.f};
      const u32x4 sel[2] = {one(r16), one(16 + r16)};
      const u32x4 selB[2] = {one(r16 < 12 ? r16 : -9), one(r16 < 12 ? 16 + r16 : -9)}, selT = one(r16 < 12 ? -9 : r16 - 12);
#pragma unroll
      for (int q = 1; q < 6; ++q)
#pragma unroll
        for (int u = 0; u < q; ++u) wp[q * (q - 1) / 2 + u] = MFMA(sel[u & 1], R[q][u >> 1], MFMA(R[u][q >> 1], sel[q & 1], z));
#pragma unroll
      for (int t = 0; t < 6; ++t) {
        const f32x4 d1 = MFMA(R[t][3], selT, MFMA(R[t][t >> 1], selB[t & 1], z));
        wp[15 + t] = w4(r16 < 12 ? 16 * t + r16 : 84 + r16, 16 * t + 4 * g4) + (g4 == 3 || r16 >= 12 ? d1 : z);
      }
      const bool same = g4 == (r16 >> 2);
      const int f1 = g4 < 2 ? 16 * g4 + 12 : 96, f2 = 16 * g4 + 44;
      wp[21] = same && g4 < 3 ? w4(f1 + (r16 & 3), f1) : z;
      wp[22] = same ? w4(f2 + (r16 & 3), f2) : z;
#pragma unroll
      for (int t = 0; t < 23; ++t) asm volatile("" : "+v"(wp[t]));
    }
    stage_idx();
    waitv<10>();
    TL(3)
    bar();
    TL(4)
    EA(2) DA4(2)
    EA(3) DA4(3)
    waitv<4>(); LDA(4) bar();
    static_assert(G0 >= 3, "start-up schedule");
#pragma unroll
    for (int n = 1; n < G0 - 2; ++n) { DA4(n + 3) LDA(n + 4) waitv<4>(); bar(); }
    LDP(0, 0) DA4(G0 + 1) DP4(0) DT LDA(G0 + 2) LDP(0, 1) waitv<9>();
    TL(5)
    bar();
    DA4(G0 + 2) DP4(1) LDA(G0 + 3) LDP(0, 2) waitv<8>(); bar();

    u32x4 fr[9][2];
#pragma unroll
    for (int t = 2; t < 6; ++t) { fr[t][0] = u32x4{0, 0, 0, 0}; fr[t][1] = u32x4{0, 0, 0, 0}; }
    float ps0 = 0.f, ps1 = 0.f, ps2 = 0.f, ps3 = 0.f;
    f32x4 gq[2][2];
    auto pm = [&](auto cc, auto u0c, auto q0c, auto u1c, auto q1c) {
      constexpr int c = decltype(cc)::value, u0 = decltype(u0c)::value, q0 = decltype(q0c)::value, u1 = decltype(u1c)::value, q1 = decltype(q1c)::value;
      const f32x4 z = f32x4{0.f, 0.f, 0.f, 0.f};
      SB
      __builtin_amdgcn_s_setprio(PM_PRIO);
      gq[c][0] = MFMA(fr[u0][0], fr[q0][0], z);
      if constexpr (u1 >= 0) gq[c][1] = MFMA(fr[u1][0], fr[q1][0], z);
      gq[c][0] = MFMA(fr[u0][1], fr[q0][1], gq[c][0]);
      if constexpr (u1 >= 0) gq[c][1] = MFMA(fr[u1][1], fr[q1][1], gq[c][1]);
      SB __builtin_amdgcn_s_setprio(PF_PRIO);
      SB
    };
    auto pf = [&](auto cc, auto w0c, auto w1c) {
      constexpr int c = decltype(cc)::value, i0 = decltype(w0c)::value, i1 = decltype(w1c)::value;
      const f32x4 w0 = wp[i0];
      ps0 = __builtin_fmaf(gq[c][0][0], w0[0], ps0); ps1 = __builtin_fmaf(gq[c][0][1], w0[1], ps1);
      ps2 = __builtin_fmaf(gq[c][0][2], w0[2], ps2); ps3 = __builtin_fmaf(gq[c][0][3], w0[3], ps3);
      if constexpr (i1 >= 0) {
        const f32x4 w1v = wp[i1];
        ps0 = __builtin_fmaf(gq[c][1][0], w1v[0], ps0); ps1 = __builtin_fmaf(gq[c][1][1], w1v[1], ps1);
        ps2 = __builtin_fmaf(gq[c][1][2], w1v[2], ps2); ps3 = __builtin_fmaf(gq[c][1][3], w1v[3], ps3);
      }
      SB
    };
#define PM(C, U0, Q0, U1, Q1) pm(IC(C), IC(U0), IC(Q0), IC(U1), IC(Q1));
#define PF(C, I0, I1) pf(IC(C), IC(I0), IC(I1));
#define WO(U, Q) (Q * (Q - 1) / 2 + U)
    float pv;
    auto total0 = [&]() {
      asm("" : "+v"(ps0)); asm("" : "+v"(ps1)); asm("" : "+v"(ps2)); asm("" : "+v"(ps3));
      pv = (ps0 + ps1) + (ps2 + ps3);
      asm volatile("" : "+v"(pv));
      ps0 = ps1 = ps2 = ps3 = 0.f;
    };
    auto total1 = [&](int s) {
      float tot = wave_sum63(pv);
      if (lane == 63 && s >= 0) red[256 + 16 * p + s] = tot;
    };
#define RDF(D, T) fr[D][0] = ldf(aF0 + T * 2048); fr[D][1] = ldf(aF1 + T * 2048);
#define RDX(D, A) fr[D][0] = ldf(A); fr[D][1] = ldf(A##k);
#define RDD(T) if (r16 < 12) { fr[6][0] = ldf(aD0 + T * 2048); fr[6][1] = ldf(aD0k + T * 2048); \
                              fr[7][0] = ldf(aD0 + (T + 1) * 2048); fr[7][1] = ldf(aD0k + (T + 1) * 2048); }
    auto xpoll = [&]() {
      GAS u64* pf = (GAS u64*)(ws + XFLAG_OFF + (bid ^ 8) * 256 + p * 64);
      const u64 tok = ((u64)thi << 32) | tlo;
      for (unsigned spins = 0; spins < (1u << 18); ++spins) {
        if (__all(__hip_atomic_load(pf, RLX_AGENT) == tok)) break;
        __builtin_amdgcn_s_sleep(1);
      }
      asm volatile("" ::: "memory");
    };
    auto steps = [&](int s, auto fac, auto nbarc, auto lastc) {
      constexpr bool fa = decltype(fac)::value, last = decltype(lastc)::value;
      constexpr int nbar = decltype(nbarc)::value;
      const int n = 3 * s + G0;
#define XA(J, I) if constexpr (fa) DA(J, I)
#define XP(G, I) if constexpr (!last) DP(G, I)
#define ENDSTEP(CNT, K) { float x0 = ps0, x1 = ps1, x2 = ps2, x3 = ps3; asm volatile("" : "+v"(x0), "+v"(x1), "+v"(x2), "+v"(x3)); ps0 = x0; ps1 = x1; ps2 = x2; ps3 = x3; } \
      STAMP(1) waitv<CNT>(); STAMP(2) if constexpr (nbar == 2 && K == 1) xpoll(); if constexpr (nbar > K) bar(); else { SB asm volatile("s_waitcnt lgkmcnt(0)" ::: "memory"); SB } STAMP(3)
      asm volatile("" : "+v"(aLo), "+v"(aHi), "+v"(aP));
      RDF(0, 0) RDF(1, 1) RDX(6, aD0) RDX(7, aD1) RDX(8, aX1)
      PM(0, 4, 5, 3, 5) XA(n + 3, 0) DP(2, 0)
      PM(1, 3, 4, 2, 5) PF(0, WO(4, 5), WO(3, 5)) XA(n + 3, 1) DP(2, 1)
      PM(0, 0, 1, 0, 6) PF(1, WO(3, 4), WO(2, 5)) total0(); if constexpr (!TOTB) total1(s - 1); XA(n + 3, 2) DP(2, 2)
      PM(1, 1, 7, 8, 8) PF(0, WO(0, 1), 15) XA(n + 3, 3) DP(2, 3) PF(1, 16, 21)
      if constexpr (fa) { LDA(G0 + 4) }
      if constexpr (!last) { LDP(1, 0) }
      ENDSTEP((fa ? 8 : 4), 0)
      RDF(2, 2) RDF(3, 3) RDD(2)
      XA(n + 4, 0) XP(0, 0)
      if constexpr (TOTB) total1(s - 1);
      PM(0, 0, 2, 1, 2) XA(n + 4, 1) XP(0, 1)
      PM(1, 0, 3, 1, 3) PF(0, WO(0, 2), WO(1, 2)) XA(n + 4, 2) XP(0, 2)
      PM(0, 2, 3, 2, 6) PF(1, WO(0, 3), WO(1, 3)) XA(n + 4, 3) XP(0, 3)
      PM(1, 3, 7, -1, -1) PF(0, WO(2, 3), 17)
      if constexpr (!last) DT
      PF(1, 18, -1)
      if constexpr (fa) { LDA(G0 + 5) }
      if constexpr (!last) { LDP(1, 1) }
      ENDSTEP(((fa ? 4 : 0) + (last ? 0 : 5)), 1)
      RDX(8, aX2) RDF(4, 4) RDF(5, 5) RDD(4)
      XA(n + 5, 0)
      PM(0, 8, 8, 0, 4) XP(1, 0) XA(n + 5, 1)
      PM(1, 1, 4, 2, 4) PF(0, 22, WO(0, 4)) XP(1, 1) XA(n + 5, 2)
      PM(0, 0, 5, 1, 5) PF(1, WO(1, 4), WO(2, 4)) XP(1, 2) XA(n + 5, 3)
      PM(1, 4, 6, 5, 7) PF(0, WO(0, 5), WO(1, 5)) XP(1, 3) PF(1, 19, 20)
      if constexpr (fa) { LDA(G0 + 6) }
      if constexpr (!last) { LDP(1, 2) }
      aLo += 12; aHi += 12; aP += 4 * OP;
      ENDSTEP(((fa ? 4 : 0) + (last ? 0 : 4)), 2)
    };
    for (int s = 0; s < S2 - 1; ++s) steps(s, T{}, IC(3), Fa{});
    if constexpr (POST == 0) steps(15, Fa{}, IC(2), T{});
    else {
      steps(S2 - 1, Fa{}, IC(2), Fa{});
      for (int s = S2; s < 15; ++s) steps(s, Fa{}, IC(0), Fa{});
      steps(15, Fa{}, IC(0), T{});
    }
    PM(0, 4, 5, 3, 5) PM(1, 3, 4, 2, 5) PF(0, WO(4, 5), WO(3, 5)) PF(1, WO(3, 4), WO(2, 5))
    total0(); total1(15);
    if (bid == 0) {
      sweep2(rsW, WFLAG_OFF + p * 2048 + lane * 16, tlo, thi);
      sweep2(rsW, WFLAG_OFF + p * 2048 + 1024 + lane * 16, tlo, thi);
      if (lane == 0) __hip_atomic_store((GAS u64*)(ws + EPOCH_OFF + p * 256), ((u64)thi << 32) | tlo, RLX_AGENT);
    }
  } else {
    LAS float* bw = (LAS float*)(lds + BW_OFF);
    const i32x4 srdW = make_srd(ws, W1T_BYTES);
    const unsigned voffW = lane * 16;
    const unsigned wbase = h * (FB * 32768) + wid * 8192;
    WSet w0, w1s, w2s, w3;
#define W_LD4(S, OFF) w_ld<0>(S, voffW, srdW, OFF); w_ld<1>(S, voffW, srdW, OFF); w_ld<2>(S, voffW, srdW, OFF); w_ld<3>(S, voffW, srdW, OFF);
    f32x4 acc[8][4];
    u64 tok;
    {
      n_wait<4>(raw);
      asm volatile("" : "+v"(ep));
      TL(1)
      WSet o; n_frag(raw, o);
      const unsigned dst = bid < 200 ? (bid >> 1) * 32768 + wid * 8192 + (bid & 1) * 4096 + lane * 16 : 0x80000000u;
#pragma unroll
      for (int c = 0; c < 4; ++c) __builtin_amdgcn_raw_buffer_store_b128(o.r[c], rsW, dst + c * 1024, 0, 16);
      const u64 e = ((u64)(unsigned)__builtin_amdgcn_readfirstlane((int)ep[1]) << 32) | (unsigned)__builtin_amdgcn_readfirstlane((int)ep[0]);
      tok = (e ^ 0x9E3779B97F4A7C15ull) * 0xBF58476D1CE4E5B9ull + 0x632BE59BD9B4E019ull;
      if (tok == e) tok = ~e;
      asm volatile("s_waitcnt vmcnt(0)" : "+v"(bq0), "+v"(bq1), "+v"(w2v), "+v"(b2v) :: "memory");
      TL(2)
      if (lane == 0) {
        __hip_atomic_store((GAS u64*)(ws + WFLAG_OFF + wid * 2048 + bid * 8), tok, RLX_AGENT);
        *(LAS u64*)(lds + TOK_OFF + wid * 8) = tok;
      }
      bw[wid * 64 + lane] = __builtin_bit_cast(float, w2v << 16);
      if (tid == 0) bw[256] = __builtin_bit_cast(float, b2v << 16);
#pragma unroll
      for (int m = 0; m < 8; ++m)
#pragma unroll
        for (int nn = 0; nn < 4; ++nn) {
#pragma unroll
          for (int r = 0; r < 4; ++r) {
            const unsigned d = (r < 2 ? bq0 : bq1)[(r & 1) * 2 + (nn >> 1)];
            acc[m][nn][r] = m < 4 ? 0.f : __builtin_bit_cast(float, (nn & 1) ? (d & 0xffff0000u) : (d << 16));
          }
          asm volatile("" : "+v"(acc[m][nn]));
        }
      bar();
      waitv<0>();
      __builtin_amdgcn_s_sleep(PDELAY);
      TL(3)
      sweep2(rsW, WFLAG_OFF + wid * 2048 + h * (2 * MAINS * 8) + (lane < FB ? lane : FB - 1) * 16, (unsigned)tok, (unsigned)(tok >> 32));
      TL(4)
    }
    W_LD4(w0, wbase) W_LD4(w1s, wbase + 4096) W_LD4(w2s, wbase + 32768)
    TL(5)
    bar();
    TL(6)
    u32x4 a[4];
    unsigned b0 = frag0, b1a = frag1;
#pragma unroll
    for (int m = 0; m < 3; ++m) a[m] = *(LAS u32x4*)(lds + b0 + m * 2048);
#define GRP(CUR, M, RD, LD) \
      mfma1(acc[M][0], CUR.r[0], a[M & 3]); SB RD SB \
      mfma1(acc[M][1], CUR.r[1], a[M & 3]); LD \
      mfma1(acc[M][2], CUR.r[2], a[M & 3]); touch(a[(M + 1) & 3]); \
      mfma1(acc[M][3], CUR.r[3], a[M & 3]); SB
#define RDK(M) if constexpr (M < 5) a[(M + 3) & 3] = *(LAS u32x4*)(lds + cb + co + (M + 3) * 2048); \
               else a[(M + 3) & 3] = *(LAS u32x4*)(lds + nb + no + (M - 5) * 2048);
    auto kstep = [&](WSet& cur, WSet& nxt, unsigned soff, unsigned cb, auto coc, unsigned nb, auto noc) {
      constexpr int co = decltype(coc)::value, no = decltype(noc)::value;
      w_wait<8>(cur);
      SB
#define G(M) GRP(cur, M, RDK(M), if constexpr ((M & 1) == 0) w_ld<M / 2>(nxt, voffW, srdW, soff);)
      G(0) G(1) G(2) G(3) G(4) G(5) G(6) G(7)
#undef G
    };
    dg_r3 = __builtin_amdgcn_s_memrealtime(); __builtin_amdgcn_s_waitcnt(0xC07F);
    dg_r4 = __builtin_amdgcn_s_memrealtime(); __builtin_amdgcn_s_waitcnt(0xC07F);
    unsigned so = wbase + 32768 + 4096;
    const unsigned wmid = MAINS * 32768 + wid * 8192;
    for (int n = 0; n < MAINS; n += 2) {
      kstep(w0, w3, so, b0, IC(0), b1a, IC(0));
      if (n == MAINS - 2) so = wmid + t_woff(0) - 28672;
      kstep(w1s, w0, so + 28672, b1a, IC(0), b0, IC(A_STAGE));
      STAMP(0) bar_nw(); STAMP(3)
      kstep(w2s, w1s, so + 32768, b0, IC(A_STAGE), b1a, IC(A_STAGE));
      b0 ^= 2 * A_STAGE;
      if (n == MAINS - 2) so = wmid + t_woff(2) - 32768 - 28672;
      kstep(w3, w2s, so + 32768 + 28672, b1a, IC(A_STAGE), b0, IC(0));
      b1a ^= 2 * A_STAGE;
      STAMP(0) bar_nw(); STAMP(3)
      so += 65536;
    }
    u32x4 px[8];
    {
      const i32x4 srdS = make_srd(ws + SLAB_OFF + (size_t)bid * SLAB_BYTES, SLAB_BYTES);
      const i32x4 srdR = make_srd(ws + SLAB_OFF + (size_t)(bid ^ 8) * SLAB_BYTES, SLAB_BYTES);
      WSet ts[NU];
      ts[0] = w0; ts[1] = w1s; ts[2] = w2s;
      auto top = [&](auto uc, auto gc) {
        constexpr int u = decltype(uc)::value, g = decltype(gc)::value, st = t_store(u, g), ld = t_load(u, g), x = t_w0(u) + t_wn(u) * g;
        if constexpr (st >= 0) {
          const f32x4 lo = acc[st >> 1][2 * (st & 1)], hi = acc[st >> 1][2 * (st & 1) + 1];
          x_st<st & 3>(u32x4{pk_bf16(lo[0], lo[1]), pk_bf16(lo[2], lo[3]), pk_bf16(hi[0], hi[1]), pk_bf16(hi[2], hi[3])}, voffW, srdS, wid * 8192 + (st >> 2) * 4096);
        }
        if constexpr (ld >= 0) x_ld<ld & 3>(px[ld], voffW, srdR, wid * 8192 + (ld >> 2) * 4096);
        if constexpr (t_wn(u) > 0) w_ld<x & 3>(ts[x >> 2], voffW, srdW, wmid + t_woff(x >> 2));
        if constexpr (t_wn(u) > 1) w_ld<(x + 1) & 3>(ts[x >> 2], voffW, srdW, wmid + t_woff(x >> 2));
      };
      auto hk = [&](auto uc) {
        constexpr int U = decltype(uc)::value;
        constexpr int co = ((MAINS + U / 4) & 3) * A_STAGE + ((U >> 1) & 1) * 8192;
        constexpr int no = ((MAINS + (U + 1) / 4) & 3) * A_STAGE + (((U + 1) >> 1) & 1) * 8192;
        const unsigned cb = (U & 1) ? frag1 : frag0, nb = (U & 1) ? frag0 : frag1;
        WSet& cur = ts[U];
        w_wait<tail_wait(U)>(cur);
        if constexpr (U == FLAG_U) {
          if (lane == 0) __hip_atomic_store((GAS u64*)(ws + XFLAG_OFF + bid * 256 + wid * 64), tok, RLX_AGENT);
        }
        SB
#define RDR(M) if constexpr (M == 0) a[3] = *(LAS u32x4*)(lds + cb + co + 3 * 2048); \
               else if constexpr (U + 1 < NU) a[M - 1] = *(LAS u32x4*)(lds + nb + no + (M - 1) * 2048);
        GRP(cur, 4, RDR(0), top(uc, IC(0));) GRP(cur, 5, RDR(1), top(uc, IC(1));) GRP(cur, 6, RDR(2), top(uc, IC(2));) GRP(cur, 7, RDR(3), top(uc, IC(3));)
      };
#define HK4(J) hk(IC(4 * J)); hk(IC(4 * J + 1)); hk(IC(4 * J + 2)); hk(IC(4 * J + 3));
      HK4(0) bar_nw(); HK4(1) bar_nw(); HK4(2) bar_nw();
      HK4((MID / 2 - 1))
      asm volatile("s_nop 7"); SB
    }
    dg_r1 = __builtin_amdgcn_s_memrealtime();
    __builtin_amdgcn_s_setprio(2);
    f32x4 ww[4];
#pragma unroll
    for (int r = 0; r < 4; ++r) ww[r] = *(LAS f32x4*)(bw + wid * 64 + g4 * 16 + r * 4);
    f32x4 wt[4];
#pragma unroll
    for (int nn = 0; nn < 4; ++nn) wt[nn] = f32x4{ww[0][nn], ww[1][nn], ww[2][nn], ww[3][nn]};
#pragma unroll
    for (int m = 0; m < 4; ++m) {
      float s = 0.f;
      if (m == 0) x_wait<6>(px[0], px[1]); else if (m == 1) x_wait<4>(px[2], px[3]); else if (m == 2) x_wait<2>(px[4], px[5]); else x_wait<0>(px[6], px[7]);
      if (m == 3) dg_r2 = __builtin_amdgcn_s_memrealtime();
      f32x2 s2 = f32x2{0.f, 0.f};
#pragma unroll
      for (int nn = 0; nn < 4; ++nn)
#pragma unroll
        for (int r = 0; r < 4; r += 2) {
          const unsigned pd = px[2 * m + (nn >> 1)][2 * (nn & 1) + (r >> 1)];
          const f32x2 x = f32x2{acc[4 + m][nn][r], acc[4 + m][nn][r + 1]} + f32x2{__builtin_bit_cast(float, pd << 16), __builtin_bit_cast(float, pd & 0xffff0000u)};
          const f32x2 t = __builtin_elementwise_fma(x * x, f32x2{-0.1029432397f, -0.1029432397f}, f32x2{-2.302208198f, -2.302208198f});
          const f32x2 z = x * t;
          const f32x2 d = f32x2{__builtin_amdgcn_exp2f(z[0]), __builtin_amdgcn_exp2f(z[1])} + f32x2{1.f, 1.f};
          const f32x2 y = x * f32x2{wt[nn][r], wt[nn][r + 1]};
          s2 = __builtin_elementwise_fma(y, f32x2{__builtin_amdgcn_rcpf(d[0]), __builtin_amdgcn_rcpf(d[1])}, s2);
        }
      s = s2[0] + s2[1];
      s += __shfl_xor(s, 16);
      s += __shfl_xor(s, 32);
      if (lane < 16) red[wid * 64 + m * 16 + lane] = s;
    }
  }
  bar();
  if (tid < 64) {
    float v = red[256 + tid] + ((red[tid] + red[64 + tid]) + (red[128 + tid] + red[192 + tid])) + *(LAS const float*)(lds + BW_OFF + 1024);
    out[sown + tid] = __float2bfloat16(v);
  }
  if (tid == 0) {
    { u64 c1 = __builtin_amdgcn_s_memtime(), r1 = __builtin_amdgcn_s_memrealtime();
      if ((bid & 31) < 2) printf("wg %d t0 %d t1 %d end %d cyc %d idx %p tab %p ws %p\n", bid, (int)(dg_r3 - dg_r0), (int)(dg_r1 - dg_r0), (int)(r1 - dg_r0), (int)(c1 - dg_c0), idx, table, ws); }
    u64* dg = (u64*)(ws + (64 << 20)) + blockIdx.x * 8;
    dg[0] = dg_c0; dg[1] = dg_r0; dg[2] = __builtin_amdgcn_s_memtime(); dg[3] = __builtin_amdgcn_s_memrealtime(); dg[4] = dg_r1; dg[5] = dg_r2; dg[6] = dg_r3; dg[7] = dg_r4;
  }
}

extern "C" void kernel_launch(void* const* d_in, const int* in_sizes, int n_in, void* d_out, int out_size, void* d_ws, size_t ws_size,
                              hipStream_t stream) {
  (void)hipFuncSetAttribute((const void*)pnn_kernel, hipFuncAttributeMaxDynamicSharedMemorySize, LDS_BYTES);
  pnn_kernel<<<NWG, 512, LDS_BYTES, stream>>>((const int*)d_in[0], (const char*)d_in[1], (u64)in_sizes[1] * 2ull, (const char*)d_in[2],
                                              (const char*)d_in[3], (const unsigned short*)d_in[4], (const unsigned short*)d_in[5],
                                              (const unsigned short*)d_in[6], (char*)d_ws, (__hip_bfloat16*)d_out);
}
```

```cpp
#include <hip/hip_runtime.h>
#include <hip/hip_bf16.h>
#include <stdint.h>
#include <type_traits>

typedef float f32x4 __attribute__((ext_vector_type(4)));
typedef float f32x2 __attribute__((ext_vector_type(2)));
typedef int i32x4 __attribute__((ext_vector_type(4)));
typedef int i32x2 __attribute__((ext_vector_type(2)));
typedef unsigned u32x4 __attribute__((ext_vector_type(4)));
typedef unsigned u32x2 __attribute__((ext_vector_type(2)));
typedef __bf16 bf16x8 __attribute__((ext_vector_type(8)));
typedef unsigned long long u64;
#define LAS __attribute__((address_space(3)))
#define GAS __attribute__((address_space(1)))

constexpr int NB = 16384, NF = 100, ND = 64, NH = 256, HF = 50;
constexpr int NWG = 256;
#define POST 1
#define PHOLD 0
#define PKG 1
#define PSLEEP 8
#define PDELAY 12
#define HIDEF 0
#define TOTB 1
#define STG 3
#define PKILL 0u
#define PM_PRIO 1
#define PF_PRIO 0
#define PNN_PRIO 1
#define MID 8
constexpr int MAINS = (NF - MID) / 2, FB = MAINS + MID, NU = 2 * MID;
constexpr int S2 = 16 - POST;
constexpr int G0 = HF - 3 * S2;
static_assert(G0 >= 2 && MAINS % 2 == 0 && MAINS <= 48, "schedule");
constexpr int FLAG_U = NU - 8;
constexpr int t_store(int u, int g) {
  if (MID >= 12) return g == 1 && u < 8 ? u : -1;
  return u < 3 ? ((g & 1) ? 2 * u + (g >> 1) : -1) : (u < 5 && g == 1 ? u + 3 : -1);
}
constexpr int t_load(int u, int g) { return u >= NU - 4 && (g & 1) ? 2 * (u - NU + 4) + (g >> 1) : -1; }
constexpr int t_iss(int v) {
  if (v < 3) return -1;
  if (v <= FLAG_U + 3) { const int x = FLAG_U - 2 - (FLAG_U + 3 - v) / 2; return v - 3 < x ? v - 3 : x; }
  return FLAG_U + (v - FLAG_U - 4) / 2;
}
constexpr int t_woff(int v) { return ((v >> 2) + ((v & 2) ? 0 : 4)) * 32768 + (v & 1) * 4096; }
constexpr int t_wn(int u) { int c = 0; for (int v = 3; v < NU; ++v) c += t_iss(v) == u; return c; }
constexpr int t_w0(int u) { for (int v = 3; v < NU; ++v) if (t_iss(v) == u) return 4 * v; return 0; }
constexpr int tail_wait(int u) {
  if (u == FLAG_U) return 0;
  int cnt = u < 3 ? 4 * (2 - u) : 0;
  for (int v = 0; v < u; ++v) {
    if (v == FLAG_U) ++cnt;
    for (int g = 0; g < 4; ++g) {
      cnt += (t_store(v, g) >= 0) + (t_load(v, g) >= 0);
      if (t_wn(v) > 0 && t_w0(v) + t_wn(v) * (g + 1) - 1 == 4 * u + 3) cnt = 0; else cnt += t_wn(v);
    }
  }
  return cnt;
}
constexpr int W1T_BYTES = NF * ND * NH * 2;
constexpr int EPOCH_OFF = W1T_BYTES;
constexpr int WFLAG_OFF = EPOCH_OFF + 1024;
constexpr int XFLAG_OFF = WFLAG_OFF + 8192;
constexpr int SLAB_OFF = XFLAG_OFF + NWG * 256;
constexpr int SLAB_BYTES = 32768;
constexpr int A_STAGE = 16384;
constexpr int P_OFF = 4 * A_STAGE;
constexpr int P_WAVE = 13 * 1024;
constexpr int WPL_OFF = 2 * A_STAGE;
constexpr int OP = 104;
constexpr int IDXO_OFF = P_OFF + 4 * P_WAVE;
constexpr int IDXP_OFF = IDXO_OFF + 64 * OP * 4;
constexpr int QP = 48;
constexpr int RED_OFF = IDXP_OFF + 64 * QP * 4;
constexpr int BW_OFF = RED_OFF + 5 * 64 * 4;
constexpr int TOK_OFF = BW_OFF + 260 * 4;
constexpr int LDS_BYTES = TOK_OFF + 32;
static_assert(LDS_BYTES <= 160 * 1024, "LDS");

__device__ __forceinline__ float bf2f(unsigned short h) { return __builtin_bit_cast(float, (unsigned)h << 16); }
__device__ __forceinline__ void dma16(unsigned voff, i32x4 srd, unsigned lds_dst) {
  asm volatile("s_mov_b32 m0, %0\n\ts_nop 0\n\tbuffer_load_dwordx4 %1, %2, 0 offen lds" :: "s"(lds_dst), "v"(voff), "s"(srd) : "memory");
}
template <int N> __device__ __forceinline__ void waitv() { asm volatile("s_waitcnt vmcnt(%0)" :: "i"(N) : "memory"); }
#define SB __builtin_amdgcn_sched_barrier(0);
__device__ __forceinline__ void bar() {
  SB asm volatile("s_waitcnt lgkmcnt(0)" ::: "memory");
  __builtin_amdgcn_s_barrier();
  asm volatile("" ::: "memory"); SB
}
__device__ __forceinline__ void bar_nw() {
  SB __builtin_amdgcn_s_barrier();
  asm volatile("" ::: "memory"); SB
}
__device__ __forceinline__ i32x4 make_srd(const void* p, unsigned bytes) {
  u64 a = (u64)p;
  i32x4 r;
  r[0] = __builtin_amdgcn_readfirstlane((int)(unsigned)a);
  r[1] = __builtin_amdgcn_readfirstlane((int)(unsigned)(a >> 32) & 0xffff);
  r[2] = (int)bytes;
  r[3] = 0x00020000;
  return r;
}
#define MFMA(a, b, c) __builtin_amdgcn_mfma_f32_16x16x32_bf16(__builtin_bit_cast(bf16x8, a), __builtin_bit_cast(bf16x8, b), c, 0, 0, 0)
#define IC(x) std::integral_constant<int, x>{}
#define RLX_AGENT __ATOMIC_RELAXED, __HIP_MEMORY_SCOPE_AGENT

struct WSet { u32x4 r[4]; };
template <int J> __device__ __forceinline__ void w_ld(WSet& s, unsigned voff, i32x4 srd, unsigned soff) {
  asm volatile("buffer_load_dwordx4 %0, %1, %2, %3 offen offset:%4 sc1" : "=v"(s.r[J]) : "v"(voff), "s"(srd), "s"(soff), "i"(J * 1024) : "memory");
}
template <int N> __device__ __forceinline__ void w_wait(WSet& s) {
  asm volatile("s_waitcnt vmcnt(%4)" : "+v"(s.r[0]), "+v"(s.r[1]), "+v"(s.r[2]), "+v"(s.r[3]) : "i"(N) : "memory");
}
template <int J> __device__ __forceinline__ void x_st(const u32x4& v, unsigned voff, i32x4 srd, unsigned soff) {
  asm volatile("buffer_store_dwordx4 %0, %1, %2, %3 offen offset:%4 sc1\n\ts_nop 1" :: "v"(v), "v"(voff), "s"(srd), "s"(soff), "i"(J * 1024) : "memory");
}
template <int J> __device__ __forceinline__ void x_ld(u32x4& v, unsigned voff, i32x4 srd, unsigned soff) {
  asm volatile("buffer_load_dwordx4 %0, %1, %2, %3 offen offset:%4 sc1" : "=v"(v) : "v"(voff), "s"(srd), "s"(soff), "i"(J * 1024) : "memory");
}
template <int N> __device__ __forceinline__ void x_wait(u32x4& c0, u32x4& c1) {
  asm volatile("s_waitcnt vmcnt(%2)" : "+v"(c0), "+v"(c1) : "i"(N) : "memory");
}
__device__ __forceinline__ unsigned pk_bf16(float lo, float hi) {
  typedef __bf16 bf16x2 __attribute__((ext_vector_type(2)));
  return __builtin_bit_cast(unsigned, bf16x2{(__bf16)lo, (__bf16)hi});
}
__device__ __forceinline__ void mfma1(f32x4& c, const u32x4& w, const u32x4& e) {
  asm volatile("v_mfma_f32_16x16x32_bf16 %0, %1, %2, %0" : "+v"(c) : "v"(w), "v"(e));
}
__device__ __forceinline__ void touch(const u32x4& x) { asm volatile("" :: "v"(x)); }
struct NSet { u32x2 r[8]; };
template <int J> __device__ __forceinline__ void n_ld(NSet& s, unsigned voff, i32x4 srd, unsigned soff) {
  asm volatile("buffer_load_dwordx2 %0, %1, %2, %3 offen offset:%4" : "=v"(s.r[J]) : "v"(voff), "s"(srd), "s"(soff), "i"(J * 512) : "memory");
}
template <int N> __device__ __forceinline__ void n_wait(NSet& s) {
  asm volatile("s_waitcnt vmcnt(%8)"
               : "+v"(s.r[0]), "+v"(s.r[1]), "+v"(s.r[2]), "+v"(s.r[3]), "+v"(s.r[4]), "+v"(s.r[5]), "+v"(s.r[6]), "+v"(s.r[7]) : "i"(N) : "memory");
}
__device__ __forceinline__ void n_frag(const NSet& s, WSet& o) {
#pragma unroll
  for (int c = 0; c < 4; ++c)
#pragma unroll
    for (int i = 0; i < 4; ++i) o.r[c][i] = __builtin_amdgcn_perm(s.r[2 * i + 1][c >> 1], s.r[2 * i][c >> 1], (c & 1) ? 0x07060302u : 0x05040100u);
}
#define N_LD8(S, V, OFF) n_ld<0>(S, V, srdN, OFF); n_ld<1>(S, V, srdN, OFF); n_ld<2>(S, V, srdN, OFF); n_ld<3>(S, V, srdN, OFF); \
                         n_ld<4>(S, V, srdN, OFF); n_ld<5>(S, V, srdN, OFF); n_ld<6>(S, V, srdN, OFF); n_ld<7>(S, V, srdN, OFF);
template <int CTRL, int RM> __device__ __forceinline__ float dpp_add(float v) {
  return v + __builtin_bit_cast(float, __builtin_amdgcn_update_dpp(0, __builtin_bit_cast(int, v), CTRL, RM, 0xf, true));
}
__device__ __forceinline__ float wave_sum63(float v) {
  v = dpp_add<0x111, 0xf>(v); v = dpp_add<0x112, 0xf>(v); v = dpp_add<0x114, 0xf>(v); v = dpp_add<0x118, 0xf>(v);
  v = dpp_add<0x142, 0xa>(v); v = dpp_add<0x143, 0xc>(v);
  return v;
}
__device__ __forceinline__ void sweep2(__amdgpu_buffer_rsrc_t rs, unsigned off, unsigned tlo, unsigned thi) {
  for (unsigned spins = 0; spins < (1u << 18); ++spins) {
    asm volatile("" ::: "memory");
    const u32x4 v = __builtin_amdgcn_raw_buffer_load_b128(rs, off, 0, 16);
    if (__all(v[0] == tlo && v[1] == thi && v[2] == tlo && v[3] == thi)) break;
    __builtin_amdgcn_s_sleep(PSLEEP);
  }
  asm volatile("" ::: "memory");
}
#define TL(i)
#define STAMP(i)

__global__ __launch_bounds__(512, 2) void pnn_kernel(const int* __restrict__ idx, const char* __restrict__ table, u64 table_bytes,
                                                     const char* __restrict__ wpg, const char* __restrict__ w1,
                                                     const unsigned short* __restrict__ b1, const unsigned short* __restrict__ w2,
                                                     const unsigned short* __restrict__ b2, char* __restrict__ ws,
                                                     __hip_bfloat16* __restrict__ out) {
  extern __shared__ __attribute__((aligned(16))) char lds_raw[];
  LAS char* lds = (LAS char*)lds_raw;
  const int tid = threadIdx.x, lane = tid & 63, wid = __builtin_amdgcn_readfirstlane(tid >> 6);
  const int bid = blockIdx.x, h = (bid >> 3) & 1;
  const int pair = (bid & 7) * 16 + (bid >> 4);
  const int sown = pair * 128 + 64 * h, spar = pair * 128 + 64 * (h ^ 1);
  const unsigned lds0 = (unsigned)(size_t)lds;
  const int r16 = lane & 15, g4 = lane >> 4;
  const unsigned frag0 = r16 * 128 + ((g4 ^ ((r16 >> 1) & 7)) << 4);
  const unsigned frag1 = frag0 ^ 64;
  LAS float* red = (LAS float*)(lds + RED_OFF);
  using T = std::true_type; using Fa = std::false_type;
  const __amdgpu_buffer_rsrc_t rsW = __builtin_amdgcn_make_buffer_rsrc(ws, 0, SLAB_OFF, 0x00020000);
  const i32x4 srdN = make_srd(w1, W1T_BYTES);

  const int p = wid - 4, r8 = lane >> 3;
  const i32x4 srdI = make_srd(idx, NB * NF * 4);
  const i32x4 srdT = make_srd(table, (unsigned)table_bytes);
  const unsigned chE = (unsigned)(((lane & 7) ^ (lane >> 4)) << 4);
  const unsigned chO = chE ^ 64;
  const unsigned ldsA = lds0 + p * 2048;
  int ia[4];
#define DA(J, I) { SB dma16((((unsigned)ia[I] << 7) + ((I & 1) ? chO : chE)), srdT, ldsA + ((J) & 3) * A_STAGE + (I >> 1) * 8192 + (I & 1) * 1024); SB }
#define DA4(J) DA(J, 0) DA(J, 1) DA(J, 2) DA(J, 3)
#define EA(J) ia[0] = e0[J]; ia[1] = e1[J]; ia[2] = e2[J]; ia[3] = e3[J];
  auto stage_idx = [&]() {
    const unsigned oo = (unsigned)(sown + 16 * p) * 400u, op = (unsigned)(spar + 16 * p) * 400u + (FB * h + 2) * 4u;
#pragma unroll
    for (int i = 0; i < 3; ++i) {
      const unsigned c = lane + 64 * i, row = c / 12, k = c % 12;
      dma16(row * 400 + (k < 11 ? op + k * 16 : oo + (MAINS + 4) * 4), srdI, lds0 + IDXP_OFF + p * (16 * QP * 4) + i * 1024);
    }
#pragma unroll
    for (int i = 0; i < 7; ++i) {
      const unsigned c = lane + 64 * i, row = c / 26, k = c % 26;
      if (i < 6 || lane < 32) dma16(oo + row * 400 + (k < 25 ? k * 16 : MAINS * 4), srdI, lds0 + IDXO_OFF + p * (16 * OP * 4) + i * 1024);
    }
  };
  NSet raw; u32x2 ep; u32x4 bq0, bq1; unsigned w2v, b2v;
  i32x4 e0, e1, e2, e3;
  if (wid < 4) {
    N_LD8(raw, g4 * 4096 + wid * 128 + r16 * 8, (unsigned)(bid < 200 ? bid : 0) * 16384u)
    asm volatile("global_load_dwordx2 %0, %1, off sc1" : "=v"(ep) : "v"(ws + EPOCH_OFF + wid * 256) : "memory");
    asm volatile("global_load_dwordx4 %0, %1, off" : "=v"(bq0) : "v"(b1 + wid * 64 + g4 * 16) : "memory");
    asm volatile("global_load_dwordx4 %0, %1, off offset:16" : "=v"(bq1) : "v"(b1 + wid * 64 + g4 * 16) : "memory");
    asm volatile("global_load_ushort %0, %1, off" : "=v"(w2v) : "v"(w2 + wid * 64 + lane) : "memory");
    asm volatile("global_load_ushort %0, %1, off" : "=v"(b2v) : "v"(b2) : "memory");
  } else {
    const unsigned eq = (unsigned)(spar + 16 * p + r8) * 400u + FB * 4u * h, eo = (unsigned)(sown + 16 * p + r8) * 400u + FB * 4u * h;
    asm volatile("buffer_load_dwordx4 %0, %1, %2, 0 offen" : "=v"(e0) : "v"(eq), "s"(srdI) : "memory");
    asm volatile("buffer_load_dwordx4 %0, %1, %2, 0 offen offset:3200" : "=v"(e1) : "v"(eq), "s"(srdI) : "memory");
    asm volatile("buffer_load_dwordx4 %0, %1, %2, 0 offen" : "=v"(e2) : "v"(eo), "s"(srdI) : "memory");
    asm volatile("buffer_load_dwordx4 %0, %1, %2, 0 offen offset:3200" : "=v"(e3) : "v"(eo), "s"(srdI) : "memory");
    const i32x4 srdP = make_srd(wpg, NF * NF * 2);
#pragma unroll
    for (int i = 0; i < 5; ++i) dma16(p * 5120 + i * 1024 + lane * 16, srdP, lds0 + WPL_OFF + p * 5120 + i * 1024);
    asm volatile("s_waitcnt vmcnt(5)" : "+v"(e0), "+v"(e1), "+v"(e2), "+v"(e3) :: "memory");
    TL(1)
    EA(0) DA4(0) EA(1) DA4(1)
  }
  int role = wid;
  asm volatile("" : "+s"(role));
  if (role >= 4) {
    __builtin_amdgcn_s_setprio(PNN_PRIO);
    unsigned aLo = lds0 + IDXP_OFF + ((16 * p + r8) * QP - 2) * 4;
    unsigned aHi = lds0 + IDXO_OFF + ((16 * p + r8) * OP + FB * h) * 4;
    unsigned aP = lds0 + IDXO_OFF + (16 * p * OP + r8) * 4;
    auto ldi = [&](unsigned a) { return *(LAS const int*)(size_t)a; };
    const unsigned ldsP = lds0 + P_OFF + p * P_WAVE;
    auto swz = [&](int row) { return (unsigned)(row * 128 + ((g4 ^ ((row >> 1) & 7)) << 4)); };
    unsigned aF0 = ldsP + frag0, aF1 = ldsP + frag1;
    unsigned aD0 = ldsP + (r16 < 12 ? frag0 : 12288 + swz(r16 - 12)), aD1 = aD0 + (r16 < 12 ? 2048 : 0);
    unsigned aX1 = ldsP + (r16 < 8 ? (r16 >> 2) * 2048 + swz(12 + (r16 & 3)) : 12288 + swz(r16 - 8));
    unsigned aX2 = ldsP + (2 + (r16 >> 2)) * 2048 + swz(12 + (r16 & 3));
    unsigned aD0k = aD0 ^ 64, aD1k = aD1 ^ 64, aX1k = aX1 ^ 64, aX2k = aX2 ^ 64;
    asm volatile("" : "+v"(aF0), "+v"(aF1), "+v"(aD0), "+v"(aD1), "+v"(aX1), "+v"(aX2), "+v"(aD0k), "+v"(aD1k), "+v"(aX1k), "+v"(aX2k));
    auto ldf = [&](unsigned a) { return *(LAS const u32x4*)(size_t)a; };
    const unsigned tailmask = r8 < 4 ? 0u : 0x80000000u;

    int ip[5];
#define LDA(J) ia[0] = ldi(aLo + 4 * (J)); ia[1] = ldi(aLo + 4 * (J) + 32 * QP); ia[2] = ldi(aHi + 4 * (J)); ia[3] = ldi(aHi + 4 * (J) + 32 * OP);
#define LDP(S, G) _Pragma("unroll") for (int i = 0; i < 4; ++i) ip[i] = ldi(aP + 4 * OP * (S) + 128 * G + 32 * i); \
                  if constexpr (G == 0) ip[4] = ldi(aP + 4 * OP * (S) + 384);
#define DP(G, I) { SB dma16((((unsigned)ip[I] << 7) + ((I & 1) ? chO : chE)) | PKILL, srdT, ldsP + G * 4096 + I * 1024); SB }
#define DT { SB dma16((((unsigned)ip[4] << 7) + chE) | tailmask | PKILL, srdT, ldsP + 12288); SB }
#define DP4(G) DP(G, 0) DP(G, 1) DP(G, 2) DP(G, 3)
    static_assert(MID == 8, "index tables");
    waitv<8>();
    TL(2)
    bar();
    const u32x2 tk = *(LAS const u32x2*)(lds + TOK_OFF + p * 8);
    const unsigned tlo = __builtin_amdgcn_readfirstlane(tk[0]), thi = __builtin_amdgcn_readfirstlane(tk[1]);
    f32x4 wp[23];
    {
      LAS const char* wl = lds + WPL_OFF;
      u32x4 R[6][4];
#pragma unroll
      for (int t = 0; t < 6; ++t)
#pragma unroll
        for (int kb = 0; kb < 4; ++kb) {
          LAS const char* rp = wl + (16 * t + r16) * 200 + 16 * g4 + 64 * kb;
          const u32x2 lo = *(LAS const u32x2*)rp, hi = *(LAS const u32x2*)(rp + 8);
          R[t][kb] = u32x4{lo[0], lo[1], hi[0], hi[1]};
        }
      auto one = [&](int k) {
        u32x4 v;
#pragma unroll
        for (int i = 0; i < 4; ++i) { const int d = k - 8 * g4 - 2 * i; v[i] = d == 0 ? 0x00003F80u : d == 1 ? 0x3F800000u : 0u; }
        return v;
      };
      auto w4 = [&](int row, int col0) {
        const u32x2 v = *(LAS const u32x2*)(wl + row * 200 + col0 * 2);
        return f32x4{__builtin_bit_cast(float, v[0] << 16), __builtin_bit_cast(float, v[0] & 0xffff0000u),
                     __builtin_bit_cast(float, v[1] << 16), __builtin_bit_cast(float, v[1] & 0xffff0000u)};
      };
      const f32x4 z = f32x4{0.f, 0.f, 0.f, 0.f};
      const u32x4 sel[2] = {one(r16), one(16 + r16)};
      const u32x4 selB[2] = {one(r16 < 12 ? r16 : -9), one(r16 < 12 ? 16 + r16 : -9)}, selT = one(r16 < 12 ? -9 : r16 - 12);
#pragma unroll
      for (int q = 1; q < 6; ++q)
#pragma unroll
        for (int u = 0; u < q; ++u) wp[q * (q - 1) / 2 + u] = MFMA(sel[u & 1], R[q][u >> 1], MFMA(R[u][q >> 1], sel[q & 1], z));
#pragma unroll
      for (int t = 0; t < 6; ++t) {
        const f32x4 d1 = MFMA(R[t][3], selT, MFMA(R[t][t >> 1], selB[t & 1], z));
        wp[15 + t] = w4(r16 < 12 ? 16 * t + r16 : 84 + r16, 16 * t + 4 * g4) + (g4 == 3 || r16 >= 12 ? d1 : z);
      }
      const bool same = g4 == (r16 >> 2);
      const int f1 = g4 < 2 ? 16 * g4 + 12 : 96, f2 = 16 * g4 + 44;
      wp[21] = same && g4 < 3 ? w4(f1 + (r16 & 3), f1) : z;
      wp[22] = same ? w4(f2 + (r16 & 3), f2) : z;
#pragma unroll
      for (int t = 0; t < 23; ++t) asm volatile("" : "+v"(wp[t]));
    }
    stage_idx();
    waitv<10>();
    TL(3)
    bar();
    TL(4)
    EA(2) DA4(2)
    EA(3) DA4(3)
    waitv<4>(); LDA(4) bar();
    static_assert(G0 >= 3, "start-up schedule");
#pragma unroll
    for (int n = 1; n < G0 - 2; ++n) { DA4(n + 3) LDA(n + 4) waitv<4>(); bar(); }
    LDP(0, 0) DA4(G0 + 1) DP4(0) DT LDA(G0 + 2) LDP(0, 1) waitv<9>();
    TL(5)
    bar();
    DA4(G0 + 2) DP4(1) LDA(G0 + 3) LDP(0, 2) waitv<8>(); bar();

    u32x4 fr[9][2];
#pragma unroll
    for (int t = 2; t < 6; ++t) { fr[t][0] = u32x4{0, 0, 0, 0}; fr[t][1] = u32x4{0, 0, 0, 0}; }
    float ps0 = 0.f, ps1 = 0.f, ps2 = 0.f, ps3 = 0.f;
    f32x4 gq[2][2];
    auto pm = [&](auto cc, auto u0c, auto q0c, auto u1c, auto q1c) {
      constexpr int c = decltype(cc)::value, u0 = decltype(u0c)::value, q0 = decltype(q0c)::value, u1 = decltype(u1c)::value, q1 = decltype(q1c)::value;
      const f32x4 z = f32x4{0.f, 0.f, 0.f, 0.f};
      SB
      __builtin_amdgcn_s_setprio(PM_PRIO);
      gq[c][0] = MFMA(fr[u0][0], fr[q0][0], z);
      if constexpr (u1 >= 0) gq[c][1] = MFMA(fr[u1][0], fr[q1][0], z);
      gq[c][0] = MFMA(fr[u0][1], fr[q0][1], gq[c][0]);
      if constexpr (u1 >= 0) gq[c][1] = MFMA(fr[u1][1], fr[q1][1], gq[c][1]);
      SB __builtin_amdgcn_s_setprio(PF_PRIO);
      SB
    };
    auto pf = [&](auto cc, auto w0c, auto w1c) {
      constexpr int c = decltype(cc)::value, i0 = decltype(w0c)::value, i1 = decltype(w1c)::value;
      const f32x4 w0 = wp[i0];
      ps0 = __builtin_fmaf(gq[c][0][0], w0[0], ps0); ps1 = __builtin_fmaf(gq[c][0][1], w0[1], ps1);
      ps2 = __builtin_fmaf(gq[c][0][2], w0[2], ps2); ps3 = __builtin_fmaf(gq[c][0][3], w0[3], ps3);
      if constexpr (i1 >= 0) {
        const f32x4 w1v = wp[i1];
        ps0 = __builtin_fmaf(gq[c][1][0], w1v[0], ps0); ps1 = __builtin_fmaf(gq[c][1][1], w1v[1], ps1);
        ps2 = __builtin_fmaf(gq[c][1][2], w1v[2], ps2); ps3 = __builtin_fmaf(gq[c][1][3], w1v[3], ps3);
      }
      SB
    };
#define PM(C, U0, Q0, U1, Q1) pm(IC(C), IC(U0), IC(Q0), IC(U1), IC(Q1));
#define PF(C, I0, I1) pf(IC(C), IC(I0), IC(I1));
#define WO(U, Q) (Q * (Q - 1) / 2 + U)
    float pv;
    auto total0 = [&]() {
      asm("" : "+v"(ps0)); asm("" : "+v"(ps1)); asm("" : "+v"(ps2)); asm("" : "+v"(ps3));
      pv = (ps0 + ps1) + (ps2 + ps3);
      asm volatile("" : "+v"(pv));
      ps0 = ps1 = ps2 = ps3 = 0.f;
    };
    auto total1 = [&](int s) {
      float tot = wave_sum63(pv);
      if (lane == 63 && s >= 0) red[256 + 16 * p + s] = tot;
    };
#define RDF(D, T) fr[D][0] = ldf(aF0 + T * 2048); fr[D][1] = ldf(aF1 + T * 2048);
#define RDX(D, A) fr[D][0] = ldf(A); fr[D][1] = ldf(A##k);
#define RDD(T) if (r16 < 12) { fr[6][0] = ldf(aD0 + T * 2048); fr[6][1] = ldf(aD0k + T * 2048); \
                              fr[7][0] = ldf(aD0 + (T + 1) * 2048); fr[7][1] = ldf(aD0k + (T + 1) * 2048); }
    auto xpoll = [&]() {
      GAS u64* pf = (GAS u64*)(ws + XFLAG_OFF + (bid ^ 8) * 256 + p * 64);
      const u64 tok = ((u64)thi << 32) | tlo;
      for (unsigned spins = 0; spins < (1u << 18); ++spins) {
        if (__all(__hip_atomic_load(pf, RLX_AGENT) == tok)) break;
        __builtin_amdgcn_s_sleep(1);
      }
      asm volatile("" ::: "memory");
    };
    auto steps = [&](int s, auto fac, auto nbarc, auto lastc) {
      constexpr bool fa = decltype(fac)::value, last = decltype(lastc)::value;
      constexpr int nbar = decltype(nbarc)::value;
      const int n = 3 * s + G0;
#define XA(J, I) if constexpr (fa) DA(J, I)
#define XP(G, I) if constexpr (!last) DP(G, I)
#define ENDSTEP(CNT, K) { float x0 = ps0, x1 = ps1, x2 = ps2, x3 = ps3; asm volatile("" : "+v"(x0), "+v"(x1), "+v"(x2), "+v"(x3)); ps0 = x0; ps1 = x1; ps2 = x2; ps3 = x3; } \
      STAMP(1) waitv<CNT>(); STAMP(2) if constexpr (nbar == 2 && K == 1) xpoll(); if constexpr (nbar > K) bar(); else { SB asm volatile("s_waitcnt lgkmcnt(0)" ::: "memory"); SB } STAMP(3)
      asm volatile("" : "+v"(aLo), "+v"(aHi), "+v"(aP));
      RDF(0, 0) RDF(1, 1) RDX(6, aD0) RDX(7, aD1) RDX(8, aX1)
      PM(0, 4, 5, 3, 5) XA(n + 3, 0) DP(2, 0)
      PM(1, 3, 4, 2, 5) PF(0, WO(4, 5), WO(3, 5)) XA(n + 3, 1) DP(2, 1)
      PM(0, 0, 1, 0, 6) PF(1, WO(3, 4), WO(2, 5)) total0(); if constexpr (!TOTB) total1(s - 1); XA(n + 3, 2) DP(2, 2)
      PM(1, 1, 7, 8, 8) PF(0, WO(0, 1), 15) XA(n + 3, 3) DP(2, 3) PF(1, 16, 21)
      if constexpr (fa) { LDA(G0 + 4) }
      if constexpr (!last) { LDP(1, 0) }
      ENDSTEP((fa ? 8 : 4), 0)
      RDF(2, 2) RDF(3, 3) RDD(2)
      XA(n + 4, 0) XP(0, 0)
      if constexpr (TOTB) total1(s - 1);
      PM(0, 0, 2, 1, 2) XA(n + 4, 1) XP(0, 1)
      PM(1, 0, 3, 1, 3) PF(0, WO(0, 2), WO(1, 2)) XA(n + 4, 2) XP(0, 2)
      PM(0, 2, 3, 2, 6) PF(1, WO(0, 3), WO(1, 3)) XA(n + 4, 3) XP(0, 3)
      PM(1, 3, 7, -1, -1) PF(0, WO(2, 3), 17)
      if constexpr (!last) DT
      PF(1, 18, -1)
      if constexpr (fa) { LDA(G0 + 5) }
      if constexpr (!last) { LDP(1, 1) }
      ENDSTEP(((fa ? 4 : 0) + (last ? 0 : 5)), 1)
      RDX(8, aX2) RDF(4, 4) RDF(5, 5) RDD(4)
      XA(n + 5, 0)
      PM(0, 8, 8, 0, 4) XP(1, 0) XA(n + 5, 1)
      PM(1, 1, 4, 2, 4) PF(0, 22, WO(0, 4)) XP(1, 1) XA(n + 5, 2)
      PM(0, 0, 5, 1, 5) PF(1, WO(1, 4), WO(2, 4)) XP(1, 2) XA(n + 5, 3)
      PM(1, 4, 6, 5, 7) PF(0, WO(0, 5), WO(1, 5)) XP(1, 3) PF(1, 19, 20)
      if constexpr (fa) { LDA(G0 + 6) }
      if constexpr (!last) { LDP(1, 2) }
      aLo += 12; aHi += 12; aP += 4 * OP;
      ENDSTEP(((fa ? 4 : 0) + (last ? 0 : 4)), 2)
    };
    for (int s = 0; s < S2 - 1; ++s) steps(s, T{}, IC(3), Fa{});
    if constexpr (POST == 0) steps(15, Fa{}, IC(2), T{});
    else {
      steps(S2 - 1, Fa{}, IC(2), Fa{});
      for (int s = S2; s < 15; ++s) steps(s, Fa{}, IC(0), Fa{});
      steps(15, Fa{}, IC(0), T{});
    }
    PM(0, 4, 5, 3, 5) PM(1, 3, 4, 2, 5) PF(0, WO(4, 5), WO(3, 5)) PF(1, WO(3, 4), WO(2, 5))
    total0(); total1(15);
    if (bid == 0) {
      sweep2(rsW, WFLAG_OFF + p * 2048 + lane * 16, tlo, thi);
      sweep2(rsW, WFLAG_OFF + p * 2048 + 1024 + lane * 16, tlo, thi);
      if (lane == 0) __hip_atomic_store((GAS u64*)(ws + EPOCH_OFF + p * 256), ((u64)thi << 32) | tlo, RLX_AGENT);
    }
  } else {
    LAS float* bw = (LAS float*)(lds + BW_OFF);
    const i32x4 srdW = make_srd(ws, W1T_BYTES);
    const unsigned voffW = lane * 16;
    const unsigned wbase = h * (FB * 32768) + wid * 8192;
    WSet w0, w1s, w2s, w3;
#define W_LD4(S, OFF) w_ld<0>(S, voffW, srdW, OFF); w_ld<1>(S, voffW, srdW, OFF); w_ld<2>(S, voffW, srdW, OFF); w_ld<3>(S, voffW, srdW, OFF);
    f32x4 acc[8][4];
    u64 tok;
    {
      n_wait<4>(raw);
      asm volatile("" : "+v"(ep));
      TL(1)
      WSet o; n_frag(raw, o);
      const unsigned dst = bid < 200 ? (bid >> 1) * 32768 + wid * 8192 + (bid & 1) * 4096 + lane * 16 : 0x80000000u;
#pragma unroll
      for (int c = 0; c < 4; ++c) __builtin_amdgcn_raw_buffer_store_b128(o.r[c], rsW, dst + c * 1024, 0, 16);
      const u64 e = ((u64)(unsigned)__builtin_amdgcn_readfirstlane((int)ep[1]) << 32) | (unsigned)__builtin_amdgcn_readfirstlane((int)ep[0]);
      tok = (e ^ 0x9E3779B97F4A7C15ull) * 0xBF58476D1CE4E5B9ull + 0x632BE59BD9B4E019ull;
      if (tok == e) tok = ~e;
      asm volatile("s_waitcnt vmcnt(0)" : "+v"(bq0), "+v"(bq1), "+v"(w2v), "+v"(b2v) :: "memory");
      TL(2)
      if (lane == 0) {
        __hip_atomic_store((GAS u64*)(ws + WFLAG_OFF + wid * 2048 + bid * 8), tok, RLX_AGENT);
        *(LAS u64*)(lds + TOK_OFF + wid * 8) = tok;
      }
      bw[wid * 64 + lane] = __builtin_bit_cast(float, w2v << 16);
      if (tid == 0) bw[256] = __builtin_bit_cast(float, b2v << 16);
#pragma unroll
      for (int m = 0; m < 8; ++m)
#pragma unroll
        for (int nn = 0; nn < 4; ++nn) {
#pragma unroll
          for (int r = 0; r < 4; ++r) {
            const unsigned d = (r < 2 ? bq0 : bq1)[(r & 1) * 2 + (nn >> 1)];
            acc[m][nn][r] = m < 4 ? 0.f : __builtin_bit_cast(float, (nn & 1) ? (d & 0xffff0000u) : (d << 16));
          }
          asm volatile("" : "+v"(acc[m][nn]));
        }
      bar();
      waitv<0>();
      __builtin_amdgcn_s_sleep(PDELAY);
      TL(3)
      sweep2(rsW, WFLAG_OFF + wid * 2048 + h * (2 * MAINS * 8) + (lane < FB ? lane : FB - 1) * 16, (unsigned)tok, (unsigned)(tok >> 32));
      TL(4)
    }
    W_LD4(w0, wbase) W_LD4(w1s, wbase + 4096) W_LD4(w2s, wbase + 32768)
    TL(5)
    bar();
    TL(6)
    u32x4 a[4];
    unsigned b0 = frag0, b1a = frag1;
#pragma unroll
    for (int m = 0; m < 3; ++m) a[m] = *(LAS u32x4*)(lds + b0 + m * 2048);
#define GRP(CUR, M, RD, LD) \
      mfma1(acc[M][0], CUR.r[0], a[M & 3]); SB RD SB \
      mfma1(acc[M][1], CUR.r[1], a[M & 3]); LD \
      mfma1(acc[M][2], CUR.r[2], a[M & 3]); touch(a[(M + 1) & 3]); \
      mfma1(acc[M][3], CUR.r[3], a[M & 3]); SB
#define RDK(M) if constexpr (M < 5) a[(M + 3) & 3] = *(LAS u32x4*)(lds + cb + co + (M + 3) * 2048); \
               else a[(M + 3) & 3] = *(LAS u32x4*)(lds + nb + no + (M - 5) * 2048);
    auto kstep = [&](WSet& cur, WSet& nxt, unsigned soff, unsigned cb, auto coc, unsigned nb, auto noc) {
      constexpr int co = decltype(coc)::value, no = decltype(noc)::value;
      w_wait<8>(cur);
      SB
#define G(M) GRP(cur, M, RDK(M), if constexpr ((M & 1) == 0) w_ld<M / 2>(nxt, voffW, srdW, soff);)
      G(0) G(1) G(2) G(3) G(4) G(5) G(6) G(7)
#undef G
    };
    unsigned so = wbase + 32768 + 4096;
    const unsigned wmid = MAINS * 32768 + wid * 8192;
    for (int n = 0; n < MAINS; n += 2) {
      kstep(w0, w3, so, b0, IC(0), b1a, IC(0));
      if (n == MAINS - 2) so = wmid + t_woff(0) - 28672;
      kstep(w1s, w0, so + 28672, b1a, IC(0), b0, IC(A_STAGE));
      STAMP(0) bar_nw(); STAMP(3)
      kstep(w2s, w1s, so + 32768, b0, IC(A_STAGE), b1a, IC(A_STAGE));
      b0 ^= 2 * A_STAGE;
      if (n == MAINS - 2) so = wmid + t_woff(2) - 32768 - 28672;
      kstep(w3, w2s, so + 32768 + 28672, b1a, IC(A_STAGE), b0, IC(0));
      b1a ^= 2 * A_STAGE;
      STAMP(0) bar_nw(); STAMP(3)
      so += 65536;
    }
    u32x4 px[8];
    {
      const i32x4 srdS = make_srd(ws + SLAB_OFF + (size_t)bid * SLAB_BYTES, SLAB_BYTES);
      const i32x4 srdR = make_srd(ws + SLAB_OFF + (size_t)(bid ^ 8) * SLAB_BYTES, SLAB_BYTES);
      WSet ts[NU];
      ts[0] = w0; ts[1] = w1s; ts[2] = w2s;
      auto top = [&](auto uc, auto gc) {
        constexpr int u = decltype(uc)::value, g = decltype(gc)::value, st = t_store(u, g), ld = t_load(u, g), x = t_w0(u) + t_wn(u) * g;
        if constexpr (st >= 0) {
          const f32x4 lo = acc[st >> 1][2 * (st & 1)], hi = acc[st >> 1][2 * (st & 1) + 1];
          x_st<st & 3>(u32x4{pk_bf16(lo[0], lo[1]), pk_bf16(lo[2], lo[3]), pk_bf16(hi[0], hi[1]), pk_bf16(hi[2], hi[3])}, voffW, srdS, wid * 8192 + (st >> 2) * 4096);
        }
        if constexpr (ld >= 0) x_ld<ld & 3>(px[ld], voffW, srdR, wid * 8192 + (ld >> 2) * 4096);
        if constexpr (t_wn(u) > 0) w_ld<x & 3>(ts[x >> 2], voffW, srdW, wmid + t_woff(x >> 2));
        if constexpr (t_wn(u) > 1) w_ld<(x + 1) & 3>(ts[x >> 2], voffW, srdW, wmid + t_woff(x >> 2));
      };
      auto hk = [&](auto uc) {
        constexpr int U = decltype(uc)::value;
        constexpr int co = ((MAINS + U / 4) & 3) * A_STAGE + ((U >> 1) & 1) * 8192;
        constexpr int no = ((MAINS + (U + 1) / 4) & 3) * A_STAGE + (((U + 1) >> 1) & 1) * 8192;
        const unsigned cb = (U & 1) ? frag1 : frag0, nb = (U & 1) ? frag0 : frag1;
        WSet& cur = ts[U];
        w_wait<tail_wait(U)>(cur);
        if constexpr (U == FLAG_U) {
          if (lane == 0) __hip_atomic_store((GAS u64*)(ws + XFLAG_OFF + bid * 256 + wid * 64), tok, RLX_AGENT);
        }
        SB
#define RDR(M) if constexpr (M == 0) a[3] = *(LAS u32x4*)(lds + cb + co + 3 * 2048); \
               else if constexpr (U + 1 < NU) a[M - 1] = *(LAS u32x4*)(lds + nb + no + (M - 1) * 2048);
        GRP(cur, 4, RDR(0), top(uc, IC(0));) GRP(cur, 5, RDR(1), top(uc, IC(1));) GRP(cur, 6, RDR(2), top(uc, IC(2));) GRP(cur, 7, RDR(3), top(uc, IC(3));)
      };
#define HK4(J) hk(IC(4 * J)); hk(IC(4 * J + 1)); hk(IC(4 * J + 2)); hk(IC(4 * J + 3));
      HK4(0) bar_nw(); HK4(1) bar_nw(); HK4(2) bar_nw();
      HK4((MID / 2 - 1))
      asm volatile("s_nop 7"); SB
    }
    __builtin_amdgcn_s_setprio(2);
    f32x4 ww[4];
#pragma unroll
    for (int r = 0; r < 4; ++r) ww[r] = *(LAS f32x4*)(bw + wid * 64 + g4 * 16 + r * 4);
    f32x4 wt[4];
#pragma unroll
    for (int nn = 0; nn < 4; ++nn) wt[nn] = f32x4{ww[0][nn], ww[1][nn], ww[2][nn], ww[3][nn]};
#pragma unroll
    for (int m = 0; m < 4; ++m) {
      float s = 0.f;
      if (m == 0) x_wait<6>(px[0], px[1]); else if (m == 1) x_wait<4>(px[2], px[3]); else if (m == 2) x_wait<2>(px[4], px[5]); else x_wait<0>(px[6], px[7]);
      f32x2 s2 = f32x2{0.f, 0.f};
#pragma unroll
      for (int nn = 0; nn < 4; ++nn)
#pragma unroll
        for (int r = 0; r < 4; r += 2) {
          const unsigned pd = px[2 * m + (nn >> 1)][2 * (nn & 1) + (r >> 1)];
          const f32x2 x = f32x2{acc[4 + m][nn][r], acc[4 + m][nn][r + 1]} + f32x2{__builtin_bit_cast(float, pd << 16), __builtin_bit_cast(float, pd & 0xffff0000u)};
          const f32x2 t = __builtin_elementwise_fma(x * x, f32x2{-0.1029432397f, -0.1029432397f}, f32x2{-2.302208198f, -2.302208198f});
          const f32x2 z = x * t;
          const f32x2 d = f32x2{__builtin_amdgcn_exp2f(z[0]), __builtin_amdgcn_exp2f(z[1])} + f32x2{1.f, 1.f};
          const f32x2 y = x * f32x2{wt[nn][r], wt[nn][r + 1]};
          s2 = __builtin_elementwise_fma(y, f32x2{__builtin_amdgcn_rcpf(d[0]), __builtin_amdgcn_rcpf(d[1])}, s2);
        }
      s = s2[0] + s2[1];
      s += __shfl_xor(s, 16);
      s += __shfl_xor(s, 32);
      if (lane < 16) red[wid * 64 + m * 16 + lane] = s;
    }
  }
  bar();
  if (tid < 64) {
    float v = red[256 + tid] + ((red[tid] + red[64 + tid]) + (red[128 + tid] + red[192 + tid])) + *(LAS const float*)(lds + BW_OFF + 1024);
    out[sown + tid] = __float2bfloat16(v);
  }
}

extern "C" void kernel_launch(void* const* d_in, const int* in_sizes, int n_in, void* d_out, int out_size, void* d_ws, size_t ws_size,
                              hipStream_t stream) {
  (void)hipFuncSetAttribute((const void*)pnn_kernel, hipFuncAttributeMaxDynamicSharedMemorySize, LDS_BYTES);
  pnn_kernel<<<NWG, 512, LDS_BYTES, stream>>>((const int*)d_in[0], (const char*)d_in[1], (u64)in_sizes[1] * 2ull, (const char*)d_in[2],
                                              (const char*)d_in[3], (const unsigned short*)d_in[4], (const unsigned short*)d_in[5],
                                              (const unsigned short*)d_in[6], (char*)d_ws, (__hip_bfloat16*)d_out);
}
```

```cpp
#include <hip/hip_runtime.h>
#include <hip/hip_bf16.h>
#include <stdint.h>
#include <type_traits>

typedef float f32x4 __attribute__((ext_vector_type(4)));
typedef float f32x2 __attribute__((ext_vector_type(2)));
typedef int i32x4 __attribute__((ext_vector_type(4)));
typedef int i32x2 __attribute__((ext_vector_type(2)));
typedef unsigned u32x4 __attribute__((ext_vector_type(4)));
typedef unsigned u32x2 __attribute__((ext_vector_type(2)));
typedef __bf16 bf16x8 __attribute__((ext_vector_type(8)));
typedef unsigned long long u64;
#define LAS __attribute__((address_space(3)))
#define GAS __attribute__((address_space(1)))

constexpr int NB = 16384, NF = 100, ND = 64, NH = 256, HF = 50;
constexpr int NWG = 256;
#define POST 1
#define PHOLD 0
#define PKG 1
#define PSLEEP 8
#define PDELAY 12
#define HIDEF 0
#define TOTB 1
#define STG 3
#define CPF 1
#define CODE_BYTES 31488
#define PKILL 0u
#define PM_PRIO 1
#define PF_PRIO 0
#define PNN_PRIO 1
#define MID 8
constexpr int MAINS = (NF - MID) / 2, FB = MAINS + MID, NU = 2 * MID;
constexpr int S2 = 16 - POST;
constexpr int G0 = HF - 3 * S2;
static_assert(G0 >= 2 && MAINS % 2 == 0 && MAINS <= 48, "schedule");
constexpr int FLAG_U = NU - 8;
constexpr int t_store(int u, int g) {
  if (MID >= 12) return g == 1 && u < 8 ? u : -1;
  return u < 3 ? ((g & 1) ? 2 * u + (g >> 1) : -1) : (u < 5 && g == 1 ? u + 3 : -1);
}
constexpr int t_load(int u, int g) { return u >= NU - 4 && (g & 1) ? 2 * (u - NU + 4) + (g >> 1) : -1; }
constexpr int t_iss(int v) {
  if (v < 3) return -1;
  if (v <= FLAG_U + 3) { const int x = FLAG_U - 2 - (FLAG_U + 3 - v) / 2; return v - 3 < x ? v - 3 : x; }
  return FLAG_U + (v - FLAG_U - 4) / 2;
}
constexpr int t_woff(int v) { return ((v >> 2) + ((v & 2) ? 0 : 4)) * 32768 + (v & 1) * 4096; }
constexpr int t_wn(int u) { int c = 0; for (int v = 3; v < NU; ++v) c += t_iss(v) == u; return c; }
constexpr int t_w0(int u) { for (int v = 3; v < NU; ++v) if (t_iss(v) == u) return 4 * v; return 0; }
constexpr int tail_wait(int u) {
  if (u == FLAG_U) return 0;
  int cnt = u < 3 ? 4 * (2 - u) : 0;
  for (int v = 0; v < u; ++v) {
    if (v == FLAG_U) ++cnt;
    for (int g = 0; g < 4; ++g) {
      cnt += (t_store(v, g) >= 0) + (t_load(v, g) >= 0);
      if (t_wn(v) > 0 && t_w0(v) + t_wn(v) * (g + 1) - 1 == 4 * u + 3) cnt = 0; else cnt += t_wn(v);
    }
  }
  return cnt;
}
constexpr int W1T_BYTES = NF * ND * NH * 2;
constexpr int EPOCH_OFF = W1T_BYTES;
constexpr int WFLAG_OFF = EPOCH_OFF + 1024;
constexpr int XFLAG_OFF = WFLAG_OFF + 8192;
constexpr int SLAB_OFF = XFLAG_OFF + NWG * 256;
constexpr int SLAB_BYTES = 32768;
constexpr int A_STAGE = 16384;
constexpr int P_OFF = 4 * A_STAGE;
constexpr int P_WAVE = 13 * 1024;
constexpr int WPL_OFF = 2 * A_STAGE;
constexpr int OP = 104;
constexpr int IDXO_OFF = P_OFF + 4 * P_WAVE;
constexpr int IDXP_OFF = IDXO_OFF + 64 * OP * 4;
constexpr int QP = 48;
constexpr int RED_OFF = IDXP_OFF + 64 * QP * 4;
constexpr int BW_OFF = RED_OFF + 5 * 64 * 4;
constexpr int TOK_OFF = BW_OFF + 260 * 4;
constexpr int LDS_BYTES = TOK_OFF + 32;
static_assert(LDS_BYTES <= 160 * 1024, "LDS");

__device__ __forceinline__ float bf2f(unsigned short h) { return __builtin_bit_cast(float, (unsigned)h << 16); }
__device__ __forceinline__ void dma16(unsigned voff, i32x4 srd, unsigned lds_dst) {
  asm volatile("s_mov_b32 m0, %0\n\ts_nop 0\n\tbuffer_load_dwordx4 %1, %2, 0 offen lds" :: "s"(lds_dst), "v"(voff), "s"(srd) : "memory");
}
template <int N> __device__ __forceinline__ void waitv() { asm volatile("s_waitcnt vmcnt(%0)" :: "i"(N) : "memory"); }
#define SB __builtin_amdgcn_sched_barrier(0);
__device__ __forceinline__ void bar() {
  SB asm volatile("s_waitcnt lgkmcnt(0)" ::: "memory");
  __builtin_amdgcn_s_barrier();
  asm volatile("" ::: "memory"); SB
}
__device__ __forceinline__ void bar_nw() {
  SB __builtin_amdgcn_s_barrier();
  asm volatile("" ::: "memory"); SB
}
__device__ __forceinline__ i32x4 make_srd(const void* p, unsigned bytes) {
  u64 a = (u64)p;
  i32x4 r;
  r[0] = __builtin_amdgcn_readfirstlane((int)(unsigned)a);
  r[1] = __builtin_amdgcn_readfirstlane((int)(unsigned)(a >> 32) & 0xffff);
  r[2] = (int)bytes;
  r[3] = 0x00020000;
  return r;
}
#define MFMA(a, b, c) __builtin_amdgcn_mfma_f32_16x16x32_bf16(__builtin_bit_cast(bf16x8, a), __builtin_bit_cast(bf16x8, b), c, 0, 0, 0)
#define IC(x) std::integral_constant<int, x>{}
#define RLX_AGENT __ATOMIC_RELAXED, __HIP_MEMORY_SCOPE_AGENT

struct WSet { u32x4 r[4]; };
template <int J> __device__ __forceinline__ void w_ld(WSet& s, unsigned voff, i32x4 srd, unsigned soff) {
  asm volatile("buffer_load_dwordx4 %0, %1, %2, %3 offen offset:%4 sc1" : "=v"(s.r[J]) : "v"(voff), "s"(srd), "s"(soff), "i"(J * 1024) : "memory");
}
template <int N> __device__ __forceinline__ void w_wait(WSet& s) {
  asm volatile("s_waitcnt vmcnt(%4)" : "+v"(s.r[0]), "+v"(s.r[1]), "+v"(s.r[2]), "+v"(s.r[3]) : "i"(N) : "memory");
}
template <int J> __device__ __forceinline__ void x_st(const u32x4& v, unsigned voff, i32x4 srd, unsigned soff) {
  asm volatile("buffer_store_dwordx4 %0, %1, %2, %3 offen offset:%4 sc1\n\ts_nop 1" :: "v"(v), "v"(voff), "s"(srd), "s"(soff), "i"(J * 1024) : "memory");
}
template <int J> __device__ __forceinline__ void x_ld(u32x4& v, unsigned voff, i32x4 srd, unsigned soff) {
  asm volatile("buffer_load_dwordx4 %0, %1, %2, %3 offen offset:%4 sc1" : "=v"(v) : "v"(voff), "s"(srd), "s"(soff), "i"(J * 1024) : "memory");
}
template <int N> __device__ __forceinline__ void x_wait(u32x4& c0, u32x4& c1) {
  asm volatile("s_waitcnt vmcnt(%2)" : "+v"(c0), "+v"(c1) : "i"(N) : "memory");
}
__device__ __forceinline__ unsigned pk_bf16(float lo, float hi) {
  typedef __bf16 bf16x2 __attribute__((ext_vector_type(2)));
  return __builtin_bit_cast(unsigned, bf16x2{(__bf16)lo, (__bf16)hi});
}
__device__ __forceinline__ void mfma1(f32x4& c, const u32x4& w, const u32x4& e) {
  asm volatile("v_mfma_f32_16x16x32_bf16 %0, %1, %2, %0" : "+v"(c) : "v"(w), "v"(e));
}
__device__ __forceinline__ void touch(const u32x4& x) { asm volatile("" :: "v"(x)); }
struct NSet { u32x2 r[8]; };
template <int J> __device__ __forceinline__ void n_ld(NSet& s, unsigned voff, i32x4 srd, unsigned soff) {
  asm volatile("buffer_load_dwordx2 %0, %1, %2, %3 offen offset:%4" : "=v"(s.r[J]) : "v"(voff), "s"(srd), "s"(soff), "i"(J * 512) : "memory");
}
template <int N> __device__ __forceinline__ void n_wait(NSet& s) {
  asm volatile("s_waitcnt vmcnt(%8)"
               : "+v"(s.r[0]), "+v"(s.r[1]), "+v"(s.r[2]), "+v"(s.r[3]), "+v"(s.r[4]), "+v"(s.r[5]), "+v"(s.r[6]), "+v"(s.r[7]) : "i"(N) : "memory");
}
__device__ __forceinline__ void n_frag(const NSet& s, WSet& o) {
#pragma unroll
  for (int c = 0; c < 4; ++c)
#pragma unroll
    for (int i = 0; i < 4; ++i) o.r[c][i] = __builtin_amdgcn_perm(s.r[2 * i + 1][c >> 1], s.r[2 * i][c >> 1], (c & 1) ? 0x07060302u : 0x05040100u);
}
#define N_LD8(S, V, OFF) n_ld<0>(S, V, srdN, OFF); n_ld<1>(S, V, srdN, OFF); n_ld<2>(S, V, srdN, OFF); n_ld<3>(S, V, srdN, OFF); \
                         n_ld<4>(S, V, srdN, OFF); n_ld<5>(S, V, srdN, OFF); n_ld<6>(S, V, srdN, OFF); n_ld<7>(S, V, srdN, OFF);
template <int CTRL, int RM> __device__ __forceinline__ float dpp_add(float v) {
  return v + __builtin_bit_cast(float, __builtin_amdgcn_update_dpp(0, __builtin_bit_cast(int, v), CTRL, RM, 0xf, true));
}
__device__ __forceinline__ float wave_sum63(float v) {
  v = dpp_add<0x111, 0xf>(v); v = dpp_add<0x112, 0xf>(v); v = dpp_add<0x114, 0xf>(v); v = dpp_add<0x118, 0xf>(v);
  v = dpp_add<0x142, 0xa>(v); v = dpp_add<0x143, 0xc>(v);
  return v;
}
__device__ __forceinline__ void sweep2(__amdgpu_buffer_rsrc_t rs, unsigned off, unsigned tlo, unsigned thi) {
  for (unsigned spins = 0; spins < (1u << 18); ++spins) {
    asm volatile("" ::: "memory");
    const u32x4 v = __builtin_amdgcn_raw_buffer_load_b128(rs, off, 0, 16);
    if (__all(v[0] == tlo && v[1] == thi && v[2] == tlo && v[3] == thi)) break;
    __builtin_amdgcn_s_sleep(PSLEEP);
  }
  asm volatile("" ::: "memory");
}
#define TL(i)
#define STAMP(i)

__global__ __launch_bounds__(512, 2) void pnn_kernel(const int* __restrict__ idx, const char* __restrict__ table, u64 table_bytes,
                                                     const char* __restrict__ wpg, const char* __restrict__ w1,
                                                     const unsigned short* __restrict__ b1, const unsigned short* __restrict__ w2,
                                                     const unsigned short* __restrict__ b2, char* __restrict__ ws,
                                                     __hip_bfloat16* __restrict__ out) {
  extern __shared__ __attribute__((aligned(16))) char lds_raw[];
  LAS char* lds = (LAS char*)lds_raw;
  const int tid = threadIdx.x, lane = tid & 63, wid = __builtin_amdgcn_readfirstlane(tid >> 6);
  const int bid = blockIdx.x, h = (bid >> 3) & 1;
  const int pair = (bid & 7) * 16 + (bid >> 4);
  const int sown = pair * 128 + 64 * h, spar = pair * 128 + 64 * (h ^ 1);
  u64 pc0;
  asm volatile("s_getpc_b64 %0" : "=s"(pc0));
  const unsigned lds0 = (unsigned)(size_t)lds;
  const int r16 = lane & 15, g4 = lane >> 4;
  const unsigned frag0 = r16 * 128 + ((g4 ^ ((r16 >> 1) & 7)) << 4);
  const unsigned frag1 = frag0 ^ 64;
  LAS float* red = (LAS float*)(lds + RED_OFF);
  using T = std::true_type; using Fa = std::false_type;
  const __amdgpu_buffer_rsrc_t rsW = __builtin_amdgcn_make_buffer_rsrc(ws, 0, SLAB_OFF, 0x00020000);
  const i32x4 srdN = make_srd(w1, W1T_BYTES);

  const int p = wid - 4, r8 = lane >> 3;
  const i32x4 srdI = make_srd(idx, NB * NF * 4);
  const i32x4 srdT = make_srd(table, (unsigned)table_bytes);
  const unsigned chE = (unsigned)(((lane & 7) ^ (lane >> 4)) << 4);
  const unsigned chO = chE ^ 64;
  const unsigned ldsA = lds0 + p * 2048;
  int ia[4];
#define DA(J, I) { SB dma16((((unsigned)ia[I] << 7) + ((I & 1) ? chO : chE)), srdT, ldsA + ((J) & 3) * A_STAGE + (I >> 1) * 8192 + (I & 1) * 1024); SB }
#define DA4(J) DA(J, 0) DA(J, 1) DA(J, 2) DA(J, 3)
#define EA(J) ia[0] = e0[J]; ia[1] = e1[J]; ia[2] = e2[J]; ia[3] = e3[J];
  auto stage_idx = [&]() {
    const unsigned oo = (unsigned)(sown + 16 * p) * 400u, op = (unsigned)(spar + 16 * p) * 400u + (FB * h + 2) * 4u;
#pragma unroll
    for (int i = 0; i < 3; ++i) {
      const unsigned c = lane + 64 * i, row = c / 12, k = c % 12;
      dma16(row * 400 + (k < 11 ? op + k * 16 : oo + (MAINS + 4) * 4), srdI, lds0 + IDXP_OFF + p * (16 * QP * 4) + i * 1024);
    }
#pragma unroll
    for (int i = 0; i < 7; ++i) {
      const unsigned c = lane + 64 * i, row = c / 26, k = c % 26;
      if (i < 6 || lane < 32) dma16(oo + row * 400 + (k < 25 ? k * 16 : MAINS * 4), srdI, lds0 + IDXO_OFF + p * (16 * OP * 4) + i * 1024);
    }
  };
  NSet raw; u32x2 ep; u32x4 bq0, bq1; unsigned w2v, b2v;
  i32x4 e0, e1, e2, e3;
  if (wid < 4) {
    N_LD8(raw, g4 * 4096 + wid * 128 + r16 * 8, (unsigned)(bid < 200 ? bid : 0) * 16384u)
    asm volatile("global_load_dwordx2 %0, %1, off sc1" : "=v"(ep) : "v"(ws + EPOCH_OFF + wid * 256) : "memory");
    asm volatile("global_load_dwordx4 %0, %1, off" : "=v"(bq0) : "v"(b1 + wid * 64 + g4 * 16) : "memory");
    asm volatile("global_load_dwordx4 %0, %1, off offset:16" : "=v"(bq1) : "v"(b1 + wid * 64 + g4 * 16) : "memory");
    asm volatile("global_load_ushort %0, %1, off" : "=v"(w2v) : "v"(w2 + wid * 64 + lane) : "memory");
    asm volatile("global_load_ushort %0, %1, off" : "=v"(b2v) : "v"(b2) : "memory");
    {
      unsigned d0, d1;
      const unsigned l = min((unsigned)((bid >> 3) * 4 + wid) * 256u, (unsigned)CODE_BYTES - 256u);
      asm volatile("s_load_dword %0, %2, %3\n\ts_load_dword %1, %2, %3 offset:128\n\ts_waitcnt lgkmcnt(0)" : "=&s"(d0), "=&s"(d1) : "s"(pc0 & ~255ull), "s"(l) : "memory");
    }
  } else {
    const unsigned eq = (unsigned)(spar + 16 * p + r8) * 400u + FB * 4u * h, eo = (unsigned)(sown + 16 * p + r8) * 400u + FB * 4u * h;
    asm volatile("buffer_load_dwordx4 %0, %1, %2, 0 offen" : "=v"(e0) : "v"(eq), "s"(srdI) : "memory");
    asm volatile("buffer_load_dwordx4 %0, %1, %2, 0 offen offset:3200" : "=v"(e1) : "v"(eq), "s"(srdI) : "memory");
    asm volatile("buffer_load_dwordx4 %0, %1, %2, 0 offen" : "=v"(e2) : "v"(eo), "s"(srdI) : "memory");
    asm volatile("buffer_load_dwordx4 %0, %1, %2, 0 offen offset:3200" : "=v"(e3) : "v"(eo), "s"(srdI) : "memory");
    const i32x4 srdP = make_srd(wpg, NF * NF * 2);
#pragma unroll
    for (int i = 0; i < 5; ++i) dma16(p * 5120 + i * 1024 + lane * 16, srdP, lds0 + WPL_OFF + p * 5120 + i * 1024);
    asm volatile("s_waitcnt vmcnt(5)" : "+v"(e0), "+v"(e1), "+v"(e2), "+v"(e3) :: "memory");
    TL(1)
    EA(0) DA4(0) EA(1) DA4(1)
  }
  int role = wid;
  asm volatile("" : "+s"(role));
  if (role >= 4) {
    __builtin_amdgcn_s_setprio(PNN_PRIO);
    unsigned aLo = lds0 + IDXP_OFF + ((16 * p + r8) * QP - 2) * 4;
    unsigned aHi = lds0 + IDXO_OFF + ((16 * p + r8) * OP + FB * h) * 4;
    unsigned aP = lds0 + IDXO_OFF + (16 * p * OP + r8) * 4;
    auto ldi = [&](unsigned a) { return *(LAS const int*)(size_t)a; };
    const unsigned ldsP = lds0 + P_OFF + p * P_WAVE;
    auto swz = [&](int row) { return (unsigned)(row * 128 + ((g4 ^ ((row >> 1) & 7)) << 4)); };
    unsigned aF0 = ldsP + frag0, aF1 = ldsP + frag1;
    unsigned aD0 = ldsP + (r16 < 12 ? frag0 : 12288 + swz(r16 - 12)), aD1 = aD0 + (r16 < 12 ? 2048 : 0);
    unsigned aX1 = ldsP + (r16 < 8 ? (r16 >> 2) * 2048 + swz(12 + (r16 & 3)) : 12288 + swz(r16 - 8));
    unsigned aX2 = ldsP + (2 + (r16 >> 2)) * 2048 + swz(12 + (r16 & 3));
    unsigned aD0k = aD0 ^ 64, aD1k = aD1 ^ 64, aX1k = aX1 ^ 64, aX2k = aX2 ^ 64;
    asm volatile("" : "+v"(aF0), "+v"(aF1), "+v"(aD0), "+v"(aD1), "+v"(aX1), "+v"(aX2), "+v"(aD0k), "+v"(aD1k), "+v"(aX1k), "+v"(aX2k));
    auto ldf = [&](unsigned a) { return *(LAS const u32x4*)(size_t)a; };
    const unsigned tailmask = r8 < 4 ? 0u : 0x80000000u;

    int ip[5];
#define LDA(J) ia[0] = ldi(aLo + 4 * (J)); ia[1] = ldi(aLo + 4 * (J) + 32 * QP); ia[2] = ldi(aHi + 4 * (J)); ia[3] = ldi(aHi + 4 * (J) + 32 * OP);
#define LDP(S, G) _Pragma("unroll") for (int i = 0; i < 4; ++i) ip[i] = ldi(aP + 4 * OP * (S) + 128 * G + 32 * i); \
                  if constexpr (G == 0) ip[4] = ldi(aP + 4 * OP * (S) + 384);
#define DP(G, I) { SB dma16((((unsigned)ip[I] << 7) + ((I & 1) ? chO : chE)) | PKILL, srdT, ldsP + G * 4096 + I * 1024); SB }
#define DT { SB dma16((((unsigned)ip[4] << 7) + chE) | tailmask | PKILL, srdT, ldsP + 12288); SB }
#define DP4(G) DP(G, 0) DP(G, 1) DP(G, 2) DP(G, 3)
    static_assert(MID == 8, "index tables");
    waitv<8>();
    TL(2)
    bar();
    const u32x2 tk = *(LAS const u32x2*)(lds + TOK_OFF + p * 8);
    const unsigned tlo = __builtin_amdgcn_readfirstlane(tk[0]), thi = __builtin_amdgcn_readfirstlane(tk[1]);
    f32x4 wp[23];
    {
      LAS const char* wl = lds + WPL_OFF;
      u32x4 R[6][4];
#pragma unroll
      for (int t = 0; t < 6; ++t)
#pragma unroll
        for (int kb = 0; kb < 4; ++kb) {
          LAS const char* rp = wl + (16 * t + r16) * 200 + 16 * g4 + 64 * kb;
          const u32x2 lo = *(LAS const u32x2*)rp, hi = *(LAS const u32x2*)(rp + 8);
          R[t][kb] = u32x4{lo[0], lo[1], hi[0], hi[1]};
        }
      auto one = [&](int k) {
        u32x4 v;
#pragma unroll
        for (int i = 0; i < 4; ++i) { const int d = k - 8 * g4 - 2 * i; v[i] = d == 0 ? 0x00003F80u : d == 1 ? 0x3F800000u : 0u; }
        return v;
      };
      auto w4 = [&](int row, int col0) {
        const u32x2 v = *(LAS const u32x2*)(wl + row * 200 + col0 * 2);
        return f32x4{__builtin_bit_cast(float, v[0] << 16), __builtin_bit_cast(float, v[0] & 0xffff0000u),
                     __builtin_bit_cast(float, v[1] << 16), __builtin_bit_cast(float, v[1] & 0xffff0000u)};
      };
      const f32x4 z = f32x4{0.f, 0.f, 0.f, 0.f};
      const u32x4 sel[2] = {one(r16), one(16 + r16)};
      const u32x4 selB[2] = {one(r16 < 12 ? r16 : -9), one(r16 < 12 ? 16 + r16 : -9)}, selT = one(r16 < 12 ? -9 : r16 - 12);
#pragma unroll
      for (int q = 1; q < 6; ++q)
#pragma unroll
        for (int u = 0; u < q; ++u) wp[q * (q - 1) / 2 + u] = MFMA(sel[u & 1], R[q][u >> 1], MFMA(R[u][q >> 1], sel[q & 1], z));
#pragma unroll
      for (int t = 0; t < 6; ++t) {
        const f32x4 d1 = MFMA(R[t][3], selT, MFMA(R[t][t >> 1], selB[t & 1], z));
        wp[15 + t] = w4(r16 < 12 ? 16 * t + r16 : 84 + r16, 16 * t + 4 * g4) + (g4 == 3 || r16 >= 12 ? d1 : z);
      }
      const bool same = g4 == (r16 >> 2);
      const int f1 = g4 < 2 ? 16 * g4 + 12 : 96, f2 = 16 * g4 + 44;
      wp[21] = same && g4 < 3 ? w4(f1 + (r16 & 3), f1) : z;
      wp[22] = same ? w4(f2 + (r16 & 3), f2) : z;
#pragma unroll
      for (int t = 0; t < 23; ++t) asm volatile("" : "+v"(wp[t]));
    }
    stage_idx();
    waitv<10>();
    TL(3)
    bar();
    TL(4)
    EA(2) DA4(2)
    EA(3) DA4(3)
    waitv<4>(); LDA(4) bar();
    static_assert(G0 >= 3, "start-up schedule");
#pragma unroll
    for (int n = 1; n < G0 - 2; ++n) { DA4(n + 3) LDA(n + 4) waitv<4>(); bar(); }
    LDP(0, 0) DA4(G0 + 1) DP4(0) DT LDA(G0 + 2) LDP(0, 1) waitv<9>();
    TL(5)
    bar();
    DA4(G0 + 2) DP4(1) LDA(G0 + 3) LDP(0, 2) waitv<8>(); bar();

    u32x4 fr[9][2];
#pragma unroll
    for (int t = 2; t < 6; ++t) { fr[t][0] = u32x4{0, 0, 0, 0}; fr[t][1] = u32x4{0, 0, 0, 0}; }
    float ps0 = 0.f, ps1 = 0.f, ps2 = 0.f, ps3 = 0.f;
    f32x4 gq[2][2];
    auto pm = [&](auto cc, auto u0c, auto q0c, auto u1c, auto q1c) {
      constexpr int c = decltype(cc)::value, u0 = decltype(u0c)::value, q0 = decltype(q0c)::value, u1 = decltype(u1c)::value, q1 = decltype(q1c)::value;
      const f32x4 z = f32x4{0.f, 0.f, 0.f, 0.f};
      SB
      __builtin_amdgcn_s_setprio(PM_PRIO);
      gq[c][0] = MFMA(fr[u0][0], fr[q0][0], z);
      if constexpr (u1 >= 0) gq[c][1] = MFMA(fr[u1][0], fr[q1][0], z);
      gq[c][0] = MFMA(fr[u0][1], fr[q0][1], gq[c][0]);
      if constexpr (u1 >= 0) gq[c][1] = MFMA(fr[u1][1], fr[q1][1], gq[c][1]);
      SB __builtin_amdgcn_s_setprio(PF_PRIO);
      SB
    };
    auto pf = [&](auto cc, auto w0c, auto w1c) {
      constexpr int c = decltype(cc)::value, i0 = decltype(w0c)::value, i1 = decltype(w1c)::value;
      const f32x4 w0 = wp[i0];
      ps0 = __builtin_fmaf(gq[c][0][0], w0[0], ps0); ps1 = __builtin_fmaf(gq[c][0][1], w0[1], ps1);
      ps2 = __builtin_fmaf(gq[c][0][2], w0[2], ps2); ps3 = __builtin_fmaf(gq[c][0][3], w0[3], ps3);
      if constexpr (i1 >= 0) {
        const f32x4 w1v = wp[i1];
        ps0 = __builtin_fmaf(gq[c][1][0], w1v[0], ps0); ps1 = __builtin_fmaf(gq[c][1][1], w1v[1], ps1);
        ps2 = __builtin_fmaf(gq[c][1][2], w1v[2], ps2); ps3 = __builtin_fmaf(gq[c][1][3], w1v[3], ps3);
      }
      SB
    };
#define PM(C, U0, Q0, U1, Q1) pm(IC(C), IC(U0), IC(Q0), IC(U1), IC(Q1));
#define PF(C, I0, I1) pf(IC(C), IC(I0), IC(I1));
#define WO(U, Q) (Q * (Q - 1) / 2 + U)
    float pv;
    auto total0 = [&]() {
      asm("" : "+v"(ps0)); asm("" : "+v"(ps1)); asm("" : "+v"(ps2)); asm("" : "+v"(ps3));
      pv = (ps0 + ps1) + (ps2 + ps3);
      asm volatile("" : "+v"(pv));
      ps0 = ps1 = ps2 = ps3 = 0.f;
    };
    auto total1 = [&](int s) {
      float tot = wave_sum63(pv);
      if (lane == 63 && s >= 0) red[256 + 16 * p + s] = tot;
    };
#define RDF(D, T) fr[D][0] = ldf(aF0 + T * 2048); fr[D][1] = ldf(aF1 + T * 2048);
#define RDX(D, A) fr[D][0] = ldf(A); fr[D][1] = ldf(A##k);
#define RDD(T) if (r16 < 12) { fr[6][0] = ldf(aD0 + T * 2048); fr[6][1] = ldf(aD0k + T * 2048); \
                              fr[7][0] = ldf(aD0 + (T + 1) * 2048); fr[7][1] = ldf(aD0k + (T + 1) * 2048); }
    auto xpoll = [&]() {
      GAS u64* pf = (GAS u64*)(ws + XFLAG_OFF + (bid ^ 8) * 256 + p * 64);
      const u64 tok = ((u64)thi << 32) | tlo;
      for (unsigned spins = 0; spins < (1u << 18); ++spins) {
        if (__all(__hip_atomic_load(pf, RLX_AGENT) == tok)) break;
        __builtin_amdgcn_s_sleep(1);
      }
      asm volatile("" ::: "memory");
    };
    auto steps = [&](int s, auto fac, auto nbarc, auto lastc) {
      constexpr bool fa = decltype(fac)::value, last = decltype(lastc)::value;
      constexpr int nbar = decltype(nbarc)::value;
      const int n = 3 * s + G0;
#define XA(J, I) if constexpr (fa) DA(J, I)
#define XP(G, I) if constexpr (!last) DP(G, I)
#define ENDSTEP(CNT, K) { float x0 = ps0, x1 = ps1, x2 = ps2, x3 = ps3; asm volatile("" : "+v"(x0), "+v"(x1), "+v"(x2), "+v"(x3)); ps0 = x0; ps1 = x1; ps2 = x2; ps3 = x3; } \
      STAMP(1) waitv<CNT>(); STAMP(2) if constexpr (nbar == 2 && K == 1) xpoll(); if constexpr (nbar > K) bar(); else { SB asm volatile("s_waitcnt lgkmcnt(0)" ::: "memory"); SB } STAMP(3)
      asm volatile("" : "+v"(aLo), "+v"(aHi), "+v"(aP));
      RDF(0, 0) RDF(1, 1) RDX(6, aD0) RDX(7, aD1) RDX(8, aX1)
      PM(0, 4, 5, 3, 5) XA(n + 3, 0) DP(2, 0)
      PM(1, 3, 4, 2, 5) PF(0, WO(4, 5), WO(3, 5)) XA(n + 3, 1) DP(2, 1)
      PM(0, 0, 1, 0, 6) PF(1, WO(3, 4), WO(2, 5)) total0(); if constexpr (!TOTB) total1(s - 1); XA(n + 3, 2) DP(2, 2)
      PM(1, 1, 7, 8, 8) PF(0, WO(0, 1), 15) XA(n + 3, 3) DP(2, 3) PF(1, 16, 21)
      if constexpr (fa) { LDA(G0 + 4) }
      if constexpr (!last) { LDP(1, 0) }
      ENDSTEP((fa ? 8 : 4), 0)
      RDF(2, 2) RDF(3, 3) RDD(2)
      XA(n + 4, 0) XP(0, 0)
      if constexpr (TOTB) total1(s - 1);
      PM(0, 0, 2, 1, 2) XA(n + 4, 1) XP(0, 1)
      PM(1, 0, 3, 1, 3) PF(0, WO(0, 2), WO(1, 2)) XA(n + 4, 2) XP(0, 2)
      PM(0, 2, 3, 2, 6) PF(1, WO(0, 3), WO(1, 3)) XA(n + 4, 3) XP(0, 3)
      PM(1, 3, 7, -1, -1) PF(0, WO(2, 3), 17)
      if constexpr (!last) DT
      PF(1, 18, -1)
      if constexpr (fa) { LDA(G0 + 5) }
      if constexpr (!last) { LDP(1, 1) }
      ENDSTEP(((fa ? 4 : 0) + (last ? 0 : 5)), 1)
      RDX(8, aX2) RDF(4, 4) RDF(5, 5) RDD(4)
      XA(n + 5, 0)
      PM(0, 8, 8, 0, 4) XP(1, 0) XA(n + 5, 1)
      PM(1, 1, 4, 2, 4) PF(0, 22, WO(0, 4)) XP(1, 1) XA(n + 5, 2)
      PM(0, 0, 5, 1, 5) PF(1, WO(1, 4), WO(2, 4)) XP(1, 2) XA(n + 5, 3)
      PM(1, 4, 6, 5, 7) PF(0, WO(0, 5), WO(1, 5)) XP(1, 3) PF(1, 19, 20)
      if constexpr (fa) { LDA(G0 + 6) }
      if constexpr (!last) { LDP(1, 2) }
      aLo += 12; aHi += 12; aP += 4 * OP;
      ENDSTEP(((fa ? 4 : 0) + (last ? 0 : 4)), 2)
    };
    for (int s = 0; s < S2 - 1; ++s) steps(s, T{}, IC(3), Fa{});
    if constexpr (POST == 0) steps(15, Fa{}, IC(2), T{});
    else {
      steps(S2 - 1, Fa{}, IC(2), Fa{});
      for (int s = S2; s < 15; ++s) steps(s, Fa{}, IC(0), Fa{});
      steps(15, Fa{}, IC(0), T{});
    }
    PM(0, 4, 5, 3, 5) PM(1, 3, 4, 2, 5) PF(0, WO(4, 5), WO(3, 5)) PF(1, WO(3, 4), WO(2, 5))
    total0(); total1(15);
    if (bid == 0) {
      sweep2(rsW, WFLAG_OFF + p * 2048 + lane * 16, tlo, thi);
      sweep2(rsW, WFLAG_OFF + p * 2048 + 1024 + lane * 16, tlo, thi);
      if (lane == 0) __hip_atomic_store((GAS u64*)(ws + EPOCH_OFF + p * 256), ((u64)thi << 32) | tlo, RLX_AGENT);
    }
  } else {
    LAS float* bw = (LAS float*)(lds + BW_OFF);
    const i32x4 srdW = make_srd(ws, W1T_BYTES);
    const unsigned voffW = lane * 16;
    const unsigned wbase = h * (FB * 32768) + wid * 8192;
    WSet w0, w1s, w2s, w3;
#define W_LD4(S, OFF) w_ld<0>(S, voffW, srdW, OFF); w_ld<1>(S, voffW, srdW, OFF); w_ld<2>(S, voffW, srdW, OFF); w_ld<3>(S, voffW, srdW, OFF);
    f32x4 acc[8][4];
    u64 tok;
    {
      n_wait<4>(raw);
      asm volatile("" : "+v"(ep));
      TL(1)
      WSet o; n_frag(raw, o);
      const unsigned dst = bid < 200 ? (bid >> 1) * 32768 + wid * 8192 + (bid & 1) * 4096 + lane * 16 : 0x80000000u;
#pragma unroll
      for (int c = 0; c < 4; ++c) __builtin_amdgcn_raw_buffer_store_b128(o.r[c], rsW, dst + c * 1024, 0, 16);
      const u64 e = ((u64)(unsigned)__builtin_amdgcn_readfirstlane((int)ep[1]) << 32) | (unsigned)__builtin_amdgcn_readfirstlane((int)ep[0]);
      tok = (e ^ 0x9E3779B97F4A7C15ull) * 0xBF58476D1CE4E5B9ull + 0x632BE59BD9B4E019ull;
      if (tok == e) tok = ~e;
      asm volatile("s_waitcnt vmcnt(0)" : "+v"(bq0), "+v"(bq1), "+v"(w2v), "+v"(b2v) :: "memory");
      TL(2)
      if (lane == 0) {
        __hip_atomic_store((GAS u64*)(ws + WFLAG_OFF + wid * 2048 + bid * 8), tok, RLX_AGENT);
        *(LAS u64*)(lds + TOK_OFF + wid * 8) = tok;
      }
      bw[wid * 64 + lane] = __builtin_bit_cast(float, w2v << 16);
      if (tid == 0) bw[256] = __builtin_bit_cast(float, b2v << 16);
#pragma unroll
      for (int m = 0; m < 8; ++m)
#pragma unroll
        for (int nn = 0; nn < 4; ++nn) {
#pragma unroll
          for (int r = 0; r < 4; ++r) {
            const unsigned d = (r < 2 ? bq0 : bq1)[(r & 1) * 2 + (nn >> 1)];
            acc[m][nn][r] = m < 4 ? 0.f : __builtin_bit_cast(float, (nn & 1) ? (d & 0xffff0000u) : (d << 16));
          }
          asm volatile("" : "+v"(acc[m][nn]));
        }
      bar();
      waitv<0>();
      __builtin_amdgcn_s_sleep(PDELAY);
      TL(3)
      sweep2(rsW, WFLAG_OFF + wid * 2048 + h * (2 * MAINS * 8) + (lane < FB ? lane : FB - 1) * 16, (unsigned)tok, (unsigned)(tok >> 32));
      TL(4)
    }
    W_LD4(w0, wbase) W_LD4(w1s, wbase + 4096) W_LD4(w2s, wbase + 32768)
    TL(5)
    bar();
    TL(6)
    u32x4 a[4];
    unsigned b0 = frag0, b1a = frag1;
#pragma unroll
    for (int m = 0; m < 3; ++m) a[m] = *(LAS u32x4*)(lds + b0 + m * 2048);
#define GRP(CUR, M, RD, LD) \
      mfma1(acc[M][0], CUR.r[0], a[M & 3]); SB RD SB \
      mfma1(acc[M][1], CUR.r[1], a[M & 3]); LD \
      mfma1(acc[M][2], CUR.r[2], a[M & 3]); touch(a[(M + 1) & 3]); \
      mfma1(acc[M][3], CUR.r[3], a[M & 3]); SB
#define RDK(M) if constexpr (M < 5) a[(M + 3) & 3] = *(LAS u32x4*)(lds + cb + co + (M + 3) * 2048); \
               else a[(M + 3) & 3] = *(LAS u32x4*)(lds + nb + no + (M - 5) * 2048);
    auto kstep = [&](WSet& cur, WSet& nxt, unsigned soff, unsigned cb, auto coc, unsigned nb, auto noc) {
      constexpr int co = decltype(coc)::value, no = decltype(noc)::value;
      w_wait<8>(cur);
      SB
#define G(M) GRP(cur, M, RDK(M), if constexpr ((M & 1) == 0) w_ld<M / 2>(nxt, voffW, srdW, soff);)
      G(0) G(1) G(2) G(3) G(4) G(5) G(6) G(7)
#undef G
    };
    unsigned so = wbase + 32768 + 4096;
    const unsigned wmid = MAINS * 32768 + wid * 8192;
    for (int n = 0; n < MAINS; n += 2) {
      kstep(w0, w3, so, b0, IC(0), b1a, IC(0));
      if (n == MAINS - 2) so = wmid + t_woff(0) - 28672;
      kstep(w1s, w0, so + 28672, b1a, IC(0), b0, IC(A_STAGE));
      STAMP(0) bar_nw(); STAMP(3)
      kstep(w2s, w1s, so + 32768, b0, IC(A_STAGE), b1a, IC(A_STAGE));
      b0 ^= 2 * A_STAGE;
      if (n == MAINS - 2) so = wmid + t_woff(2) - 32768 - 28672;
      kstep(w3, w2s, so + 32768 + 28672, b1a, IC(A_STAGE), b0, IC(0));
      b1a ^= 2 * A_STAGE;
      STAMP(0) bar_nw(); STAMP(3)
      so += 65536;
    }
    u32x4 px[8];
    {
      const i32x4 srdS = make_srd(ws + SLAB_OFF + (size_t)bid * SLAB_BYTES, SLAB_BYTES);
      const i32x4 srdR = make_srd(ws + SLAB_OFF + (size_t)(bid ^ 8) * SLAB_BYTES, SLAB_BYTES);
      WSet ts[NU];
      ts[0] = w0; ts[1] = w1s; ts[2] = w2s;
      auto top = [&](auto uc, auto gc) {
        constexpr int u = decltype(uc)::value, g = decltype(gc)::value, st = t_store(u, g), ld = t_load(u, g), x = t_w0(u) + t_wn(u) * g;
        if constexpr (st >= 0) {
          const f32x4 lo = acc[st >> 1][2 * (st & 1)], hi = acc[st >> 1][2 * (st & 1) + 1];
          x_st<st & 3>(u32x4{pk_bf16(lo[0], lo[1]), pk_bf16(lo[2], lo[3]), pk_bf16(hi[0], hi[1]), pk_bf16(hi[2], hi[3])}, voffW, srdS, wid * 8192 + (st >> 2) * 4096);
        }
        if constexpr (ld >= 0) x_ld<ld & 3>(px[ld], voffW, srdR, wid * 8192 + (ld >> 2) * 4096);
        if constexpr (t_wn(u) > 0) w_ld<x & 3>(ts[x >> 2], voffW, srdW, wmid + t_woff(x >> 2));
        if constexpr (t_wn(u) > 1) w_ld<(x + 1) & 3>(ts[x >> 2], voffW, srdW, wmid + t_woff(x >> 2));
      };
      auto hk = [&](auto uc) {
        constexpr int U = decltype(uc)::value;
        constexpr int co = ((MAINS + U / 4) & 3) * A_STAGE + ((U >> 1) & 1) * 8192;
        constexpr int no = ((MAINS + (U + 1) / 4) & 3) * A_STAGE + (((U + 1) >> 1) & 1) * 8192;
        const unsigned cb = (U & 1) ? frag1 : frag0, nb = (U & 1) ? frag0 : frag1;
        WSet& cur = ts[U];
        w_wait<tail_wait(U)>(cur);
        if constexpr (U == FLAG_U) {
          if (lane == 0) __hip_atomic_store((GAS u64*)(ws + XFLAG_OFF + bid * 256 + wid * 64), tok, RLX_AGENT);
        }
        SB
#define RDR(M) if constexpr (M == 0) a[3] = *(LAS u32x4*)(lds + cb + co + 3 * 2048); \
               else if constexpr (U + 1 < NU) a[M - 1] = *(LAS u32x4*)(lds + nb + no + (M - 1) * 2048);
        GRP(cur, 4, RDR(0), top(uc, IC(0));) GRP(cur, 5, RDR(1), top(uc, IC(1));) GRP(cur, 6, RDR(2), top(uc, IC(2));) GRP(cur, 7, RDR(3), top(uc, IC(3));)
      };
#define HK4(J) hk(IC(4 * J)); hk(IC(4 * J + 1)); hk(IC(4 * J + 2)); hk(IC(4 * J + 3));
      HK4(0) bar_nw(); HK4(1) bar_nw(); HK4(2) bar_nw();
      HK4((MID / 2 - 1))
      asm volatile("s_nop 7"); SB
    }
    __builtin_amdgcn_s_setprio(2);
    f32x4 ww[4];
#pragma unroll
    for (int r = 0; r < 4; ++r) ww[r] = *(LAS f32x4*)(bw + wid * 64 + g4 * 16 + r * 4);
    f32x4 wt[4];
#pragma unroll
    for (int nn = 0; nn < 4; ++nn) wt[nn] = f32x4{ww[0][nn], ww[1][nn], ww[2][nn], ww[3][nn]};
#pragma unroll
    for (int m = 0; m < 4; ++m) {
      float s = 0.f;
      if (m == 0) x_wait<6>(px[0], px[1]); else if (m == 1) x_wait<4>(px[2], px[3]); else if (m == 2) x_wait<2>(px[4], px[5]); else x_wait<0>(px[6], px[7]);
      f32x2 s2 = f32x2{0.f, 0.f};
#pragma unroll
      for (int nn = 0; nn < 4; ++nn)
#pragma unroll
        for (int r = 0; r < 4; r += 2) {
          const unsigned pd = px[2 * m + (nn >> 1)][2 * (nn & 1) + (r >> 1)];
          const f32x2 x = f32x2{acc[4 + m][nn][r], acc[4 + m][nn][r + 1]} + f32x2{__builtin_bit_cast(float, pd << 16), __builtin_bit_cast(float, pd & 0xffff0000u)};
          const f32x2 t = __builtin_elementwise_fma(x * x, f32x2{-0.1029432397f, -0.1029432397f}, f32x2{-2.302208198f, -2.302208198f});
          const f32x2 z = x * t;
          const f32x2 d = f32x2{__builtin_amdgcn_exp2f(z[0]), __builtin_amdgcn_exp2f(z[1])} + f32x2{1.f, 1.f};
          const f32x2 y = x * f32x2{wt[nn][r], wt[nn][r + 1]};
          s2 = __builtin_elementwise_fma(y, f32x2{__builtin_amdgcn_rcpf(d[0]), __builtin_amdgcn_rcpf(d[1])}, s2);
        }
      s = s2[0] + s2[1];
      s += __shfl_xor(s, 16);
      s += __shfl_xor(s, 32);
      if (lane < 16) red[wid * 64 + m * 16 + lane] = s;
    }
  }
  bar();
  if (tid < 64) {
    float v = red[256 + tid] + ((red[tid] + red[64 + tid]) + (red[128 + tid] + red[192 + tid])) + *(LAS const float*)(lds + BW_OFF + 1024);
    out[sown + tid] = __float2bfloat16(v);
  }
}

extern "C" void kernel_launch(void* const* d_in, const int* in_sizes, int n_in, void* d_out, int out_size, void* d_ws, size_t ws_size,
                              hipStream_t stream) {
  (void)hipFuncSetAttribute((const void*)pnn_kernel, hipFuncAttributeMaxDynamicSharedMemorySize, LDS_BYTES);
  pnn_kernel<<<NWG, 512, LDS_BYTES, stream>>>((const int*)d_in[0], (const char*)d_in[1], (u64)in_sizes[1] * 2ull, (const char*)d_in[2],
                                              (const char*)d_in[3], (const unsigned short*)d_in[4], (const unsigned short*)d_in[5],
                                              (const unsigned short*)d_in[6], (char*)d_ws, (__hip_bfloat16*)d_out);
}
```

```cpp
#include <hip/hip_runtime.h>
#include <hip/hip_bf16.h>
#include <stdint.h>
#include <type_traits>

typedef float f32x4 __attribute__((ext_vector_type(4)));
typedef float f32x2 __attribute__((ext_vector_type(2)));
typedef int i32x4 __attribute__((ext_vector_type(4)));
typedef int i32x2 __attribute__((ext_vector_type(2)));
typedef unsigned u32x4 __attribute__((ext_vector_type(4)));
typedef unsigned u32x2 __attribute__((ext_vector_type(2)));
typedef __bf16 bf16x8 __attribute__((ext_vector_type(8)));
typedef unsigned long long u64;
#define LAS __attribute__((address_space(3)))
#define GAS __attribute__((address_space(1)))

constexpr int NB = 16384, NF = 100, ND = 64, NH = 256, HF = 50;
constexpr int NWG = 256;
#define POST 1
#define PHOLD 0
#define PKG 1
#define PSLEEP 8
#define PDELAY 12
#define HIDEF 0
#define TOTB 1
#define STG 3
#define CPF 1
#define CODE_BYTES 31488
#define PTOUCH 1
#define PKILL 0u
#define PM_PRIO 1
#define PF_PRIO 0
#define PNN_PRIO 1
#define MID 8
constexpr int MAINS = (NF - MID) / 2, FB = MAINS + MID, NU = 2 * MID;
constexpr int S2 = 16 - POST;
constexpr int G0 = HF - 3 * S2;
static_assert(G0 >= 2 && MAINS % 2 == 0 && MAINS <= 48, "schedule");
constexpr int FLAG_U = NU - 8;
constexpr int t_store(int u, int g) {
  if (MID >= 12) return g == 1 && u < 8 ? u : -1;
  return u < 3 ? ((g & 1) ? 2 * u + (g >> 1) : -1) : (u < 5 && g == 1 ? u + 3 : -1);
}
constexpr int t_load(int u, int g) { return u >= NU - 4 && (g & 1) ? 2 * (u - NU + 4) + (g >> 1) : -1; }
constexpr int t_iss(int v) {
  if (v < 3) return -1;
  if (v <= FLAG_U + 3) { const int x = FLAG_U - 2 - (FLAG_U + 3 - v) / 2; return v - 3 < x ? v - 3 : x; }
  return FLAG_U + (v - FLAG_U - 4) / 2;
}
constexpr int t_woff(int v) { return ((v >> 2) + ((v & 2) ? 0 : 4)) * 32768 + (v & 1) * 4096; }
constexpr int t_wn(int u) { int c = 0; for (int v = 3; v < NU; ++v) c += t_iss(v) == u; return c; }
constexpr int t_w0(int u) { for (int v = 3; v < NU; ++v) if (t_iss(v) == u) return 4 * v; return 0; }
constexpr int tail_wait(int u) {
  if (u == FLAG_U) return 0;
  int cnt = u < 3 ? 4 * (2 - u) : 0;
  for (int v = 0; v < u; ++v) {
    if (v == FLAG_U) ++cnt;
    for (int g = 0; g < 4; ++g) {
      cnt += (t_store(v, g) >= 0) + (t_load(v, g) >= 0);
      if (t_wn(v) > 0 && t_w0(v) + t_wn(v) * (g + 1) - 1 == 4 * u + 3) cnt = 0; else cnt += t_wn(v);
    }
  }
  return cnt;
}
constexpr int W1T_BYTES = NF * ND * NH * 2;
constexpr int EPOCH_OFF = W1T_BYTES;
constexpr int WFLAG_OFF = EPOCH_OFF + 1024;
constexpr int XFLAG_OFF = WFLAG_OFF + 8192;
constexpr int SLAB_OFF = XFLAG_OFF + NWG * 256;
constexpr int SLAB_BYTES = 32768;
constexpr int A_STAGE = 16384;
constexpr int P_OFF = 4 * A_STAGE;
constexpr int P_WAVE = 13 * 1024;
constexpr int WPL_OFF = 2 * A_STAGE;
constexpr int OP = 104;
constexpr int IDXO_OFF = P_OFF + 4 * P_WAVE;
constexpr int IDXP_OFF = IDXO_OFF + 64 * OP * 4;
constexpr int QP = 48;
constexpr int RED_OFF = IDXP_OFF + 64 * QP * 4;
constexpr int BW_OFF = RED_OFF + 5 * 64 * 4;
constexpr int TOK_OFF = BW_OFF + 260 * 4;
constexpr int LDS_BYTES = TOK_OFF + 32;
static_assert(LDS_BYTES <= 160 * 1024, "LDS");

__device__ __forceinline__ float bf2f(unsigned short h) { return __builtin_bit_cast(float, (unsigned)h << 16); }
__device__ __forceinline__ void dma16(unsigned voff, i32x4 srd, unsigned lds_dst) {
  asm volatile("s_mov_b32 m0, %0\n\ts_nop 0\n\tbuffer_load_dwordx4 %1, %2, 0 offen lds" :: "s"(lds_dst), "v"(voff), "s"(srd) : "memory");
}
template <int N> __device__ __forceinline__ void waitv() { asm volatile("s_waitcnt vmcnt(%0)" :: "i"(N) : "memory"); }
#define SB __builtin_amdgcn_sched_barrier(0);
__device__ __forceinline__ void bar() {
  SB asm volatile("s_waitcnt lgkmcnt(0)" ::: "memory");
  __builtin_amdgcn_s_barrier();
  asm volatile("" ::: "memory"); SB
}
__device__ __forceinline__ void bar_nw() {
  SB __builtin_amdgcn_s_barrier();
  asm volatile("" ::: "memory"); SB
}
__device__ __forceinline__ i32x4 make_srd(const void* p, unsigned bytes) {
  u64 a = (u64)p;
  i32x4 r;
  r[0] = __builtin_amdgcn_readfirstlane((int)(unsigned)a);
  r[1] = __builtin_amdgcn_readfirstlane((int)(unsigned)(a >> 32) & 0xffff);
  r[2] = (int)bytes;
  r[3] = 0x00020000;
  return r;
}
#define MFMA(a, b, c) __builtin_amdgcn_mfma_f32_16x16x32_bf16(__builtin_bit_cast(bf16x8, a), __builtin_bit_cast(bf16x8, b), c, 0, 0, 0)
#define IC(x) std::integral_constant<int, x>{}
#define RLX_AGENT __ATOMIC_RELAXED, __HIP_MEMORY_SCOPE_AGENT

struct WSet { u32x4 r[4]; };
template <int J> __device__ __forceinline__ void w_ld(WSet& s, unsigned voff, i32x4 srd, unsigned soff) {
  asm volatile("buffer_load_dwordx4 %0, %1, %2, %3 offen offset:%4 sc1" : "=v"(s.r[J]) : "v"(voff), "s"(srd), "s"(soff), "i"(J * 1024) : "memory");
}
template <int N> __device__ __forceinline__ void w_wait(WSet& s) {
  asm volatile("s_waitcnt vmcnt(%4)" : "+v"(s.r[0]), "+v"(s.r[1]), "+v"(s.r[2]), "+v"(s.r[3]) : "i"(N) : "memory");
}
template <int J> __device__ __forceinline__ void x_st(const u32x4& v, unsigned voff, i32x4 srd, unsigned soff) {
  asm volatile("buffer_store_dwordx4 %0, %1, %2, %3 offen offset:%4 sc1\n\ts_nop 1" :: "v"(v), "v"(voff), "s"(srd), "s"(soff), "i"(J * 1024) : "memory");
}
template <int J> __device__ __forceinline__ void x_ld(u32x4& v, unsigned voff, i32x4 srd, unsigned soff) {
  asm volatile("buffer_load_dwordx4 %0, %1, %2, %3 offen offset:%4 sc1" : "=v"(v) : "v"(voff), "s"(srd), "s"(soff), "i"(J * 1024) : "memory");
}
template <int N> __device__ __forceinline__ void x_wait(u32x4& c0, u32x4& c1) {
  asm volatile("s_waitcnt vmcnt(%2)" : "+v"(c0), "+v"(c1) : "i"(N) : "memory");
}
__device__ __forceinline__ unsigned pk_bf16(float lo, float hi) {
  typedef __bf16 bf16x2 __attribute__((ext_vector_type(2)));
  return __builtin_bit_cast(unsigned, bf16x2{(__bf16)lo, (__bf16)hi});
}
__device__ __forceinline__ void mfma1(f32x4& c, const u32x4& w, const u32x4& e) {
  asm volatile("v_mfma_f32_16x16x32_bf16 %0, %1, %2, %0" : "+v"(c) : "v"(w), "v"(e));
}
__device__ __forceinline__ void touch(const u32x4& x) { asm volatile("" :: "v"(x)); }
struct NSet { u32x2 r[8]; };
template <int J> __device__ __forceinline__ void n_ld(NSet& s, unsigned voff, i32x4 srd, unsigned soff) {
  asm volatile("buffer_load_dwordx2 %0, %1, %2, %3 offen offset:%4" : "=v"(s.r[J]) : "v"(voff), "s"(srd), "s"(soff), "i"(J * 512) : "memory");
}
template <int N> __device__ __forceinline__ void n_wait(NSet& s) {
  asm volatile("s_waitcnt vmcnt(%8)"
               : "+v"(s.r[0]), "+v"(s.r[1]), "+v"(s.r[2]), "+v"(s.r[3]), "+v"(s.r[4]), "+v"(s.r[5]), "+v"(s.r[6]), "+v"(s.r[7]) : "i"(N) : "memory");
}
__device__ __forceinline__ void n_frag(const NSet& s, WSet& o) {
#pragma unroll
  for (int c = 0; c < 4; ++c)
#pragma unroll
    for (int i = 0; i < 4; ++i) o.r[c][i] = __builtin_amdgcn_perm(s.r[2 * i + 1][c >> 1], s.r[2 * i][c >> 1], (c & 1) ? 0x07060302u : 0x05040100u);
}
#define N_LD8(S, V, OFF) n_ld<0>(S, V, srdN, OFF); n_ld<1>(S, V, srdN, OFF); n_ld<2>(S, V, srdN, OFF); n_ld<3>(S, V, srdN, OFF); \
                         n_ld<4>(S, V, srdN, OFF); n_ld<5>(S, V, srdN, OFF); n_ld<6>(S, V, srdN, OFF); n_ld<7>(S, V, srdN, OFF);
template <int CTRL, int RM> __device__ __forceinline__ float dpp_add(float v) {
  return v + __builtin_bit_cast(float, __builtin_amdgcn_update_dpp(0, __builtin_bit_cast(int, v), CTRL, RM, 0xf, true));
}
__device__ __forceinline__ float wave_sum63(float v) {
  v = dpp_add<0x111, 0xf>(v); v = dpp_add<0x112, 0xf>(v); v = dpp_add<0x114, 0xf>(v); v = dpp_add<0x118, 0xf>(v);
  v = dpp_add<0x142, 0xa>(v); v = dpp_add<0x143, 0xc>(v);
  return v;
}
__device__ __forceinline__ void sweep2(__amdgpu_buffer_rsrc_t rs, unsigned off, unsigned tlo, unsigned thi) {
  for (unsigned spins = 0; spins < (1u << 18); ++spins) {
    asm volatile("" ::: "memory");
    const u32x4 v = __builtin_amdgcn_raw_buffer_load_b128(rs, off, 0, 16);
    if (__all(v[0] == tlo && v[1] == thi && v[2] == tlo && v[3] == thi)) break;
    __builtin_amdgcn_s_sleep(PSLEEP);
  }
  asm volatile("" ::: "memory");
}
#define TL(i)
#define STAMP(i)

__global__ __launch_bounds__(512, 2) void pnn_kernel(const int* __restrict__ idx, const char* __restrict__ table, u64 table_bytes,
                                                     const char* __restrict__ wpg, const char* __restrict__ w1,
                                                     const unsigned short* __restrict__ b1, const unsigned short* __restrict__ w2,
                                                     const unsigned short* __restrict__ b2, char* __restrict__ ws,
                                                     __hip_bfloat16* __restrict__ out) {
  extern __shared__ __attribute__((aligned(16))) char lds_raw[];
  LAS char* lds = (LAS char*)lds_raw;
  const int tid = threadIdx.x, lane = tid & 63, wid = __builtin_amdgcn_readfirstlane(tid >> 6);
  const int bid = blockIdx.x, h = (bid >> 3) & 1;
  const int pair = (bid & 7) * 16 + (bid >> 4);
  const int sown = pair * 128 + 64 * h, spar = pair * 128 + 64 * (h ^ 1);
  u64 pc0;
  asm volatile("s_getpc_b64 %0" : "=s"(pc0));
  const unsigned lds0 = (unsigned)(size_t)lds;
  const int r16 = lane & 15, g4 = lane >> 4;
  const unsigned frag0 = r16 * 128 + ((g4 ^ ((r16 >> 1) & 7)) << 4);
  const unsigned frag1 = frag0 ^ 64;
  LAS float* red = (LAS float*)(lds + RED_OFF);
  using T = std::true_type; using Fa = std::false_type;
  const __amdgpu_buffer_rsrc_t rsW = __builtin_amdgcn_make_buffer_rsrc(ws, 0, SLAB_OFF, 0x00020000);
  const i32x4 srdN = make_srd(w1, W1T_BYTES);

  const int p = wid - 4, r8 = lane >> 3;
  const i32x4 srdI = make_srd(idx, NB * NF * 4);
  const i32x4 srdT = make_srd(table, (unsigned)table_bytes);
  const unsigned chE = (unsigned)(((lane & 7) ^ (lane >> 4)) << 4);
  const unsigned chO = chE ^ 64;
  const unsigned ldsA = lds0 + p * 2048;
  int ia[4];
#define DA(J, I) { SB dma16((((unsigned)ia[I] << 7) + ((I & 1) ? chO : chE)), srdT, ldsA + ((J) & 3) * A_STAGE + (I >> 1) * 8192 + (I & 1) * 1024); SB }
#define DA4(J) DA(J, 0) DA(J, 1) DA(J, 2) DA(J, 3)
#define EA(J) ia[0] = e0[J]; ia[1] = e1[J]; ia[2] = e2[J]; ia[3] = e3[J];
  auto stage_idx = [&]() {
    const unsigned oo = (unsigned)(sown + 16 * p) * 400u, op = (unsigned)(spar + 16 * p) * 400u + (FB * h + 2) * 4u;
#pragma unroll
    for (int i = 0; i < 3; ++i) {
      const unsigned c = lane + 64 * i, row = c / 12, k = c % 12;
      dma16(row * 400 + (k < 11 ? op + k * 16 : oo + (MAINS + 4) * 4), srdI, lds0 + IDXP_OFF + p * (16 * QP * 4) + i * 1024);
    }
#pragma unroll
    for (int i = 0; i < 7; ++i) {
      const unsigned c = lane + 64 * i, row = c / 26, k = c % 26;
      if (i < 6 || lane < 32) dma16(oo + row * 400 + (k < 25 ? k * 16 : MAINS * 4), srdI, lds0 + IDXO_OFF + p * (16 * OP * 4) + i * 1024);
    }
  };
  NSet raw; u32x2 ep; u32x4 bq0, bq1; unsigned w2v, b2v, pt = 0;
  i32x4 e0, e1, e2, e3;
  if (wid < 4) {
    N_LD8(raw, g4 * 4096 + wid * 128 + r16 * 8, (unsigned)(bid < 200 ? bid : 0) * 16384u)
    asm volatile("global_load_dwordx2 %0, %1, off sc1" : "=v"(ep) : "v"(ws + EPOCH_OFF + wid * 256) : "memory");
    asm volatile("global_load_dwordx4 %0, %1, off" : "=v"(bq0) : "v"(b1 + wid * 64 + g4 * 16) : "memory");
    asm volatile("global_load_dwordx4 %0, %1, off offset:16" : "=v"(bq1) : "v"(b1 + wid * 64 + g4 * 16) : "memory");
    asm volatile("global_load_ushort %0, %1, off" : "=v"(w2v) : "v"(w2 + wid * 64 + lane) : "memory");
    asm volatile("global_load_ushort %0, %1, off" : "=v"(b2v) : "v"(b2) : "memory");
    {
      const int k = r16;
      const char* a = k < 8 ? table + (size_t)k * ((table_bytes >> 10) << 7) + (bid & 63) * 4096
                    : k == 8 ? ws : k == 9 ? ws + (2 << 20) : k == 10 ? ws + SLAB_OFF + (size_t)bid * SLAB_BYTES
                    : k == 11 ? ws + SLAB_OFF + (size_t)(bid ^ 8) * SLAB_BYTES : k == 12 ? (const char*)(out + sown) : table + table_bytes - 128;
      asm volatile("global_load_dword %0, %1, off" : "=v"(pt) : "v"(a) : "memory");
    }
    {
      unsigned d0, d1;
      const unsigned l = min((unsigned)((bid >> 3) * 4 + wid) * 256u, (unsigned)CODE_BYTES - 256u);
      asm volatile("s_load_dword %0, %2, %3\n\ts_load_dword %1, %2, %3 offset:128\n\ts_waitcnt lgkmcnt(0)" : "=&s"(d0), "=&s"(d1) : "s"(pc0 & ~255ull), "s"(l) : "memory");
    }
  } else {
    const unsigned eq = (unsigned)(spar + 16 * p + r8) * 400u + FB * 4u * h, eo = (unsigned)(sown + 16 * p + r8) * 400u + FB * 4u * h;
    asm volatile("buffer_load_dwordx4 %0, %1, %2, 0 offen" : "=v"(e0) : "v"(eq), "s"(srdI) : "memory");
    asm volatile("buffer_load_dwordx4 %0, %1, %2, 0 offen offset:3200" : "=v"(e1) : "v"(eq), "s"(srdI) : "memory");
    asm volatile("buffer_load_dwordx4 %0, %1, %2, 0 offen" : "=v"(e2) : "v"(eo), "s"(srdI) : "memory");
    asm volatile("buffer_load_dwordx4 %0, %1, %2, 0 offen offset:3200" : "=v"(e3) : "v"(eo), "s"(srdI) : "memory");
    const i32x4 srdP = make_srd(wpg, NF * NF * 2);
#pragma unroll
    for (int i = 0; i < 5; ++i) dma16(p * 5120 + i * 1024 + lane * 16, srdP, lds0 + WPL_OFF + p * 5120 + i * 1024);
    asm volatile("s_waitcnt vmcnt(5)" : "+v"(e0), "+v"(e1), "+v"(e2), "+v"(e3) :: "memory");
    TL(1)
    EA(0) DA4(0) EA(1) DA4(1)
  }
  int role = wid;
  asm volatile("" : "+s"(role));
  if (role >= 4) {
    __builtin_amdgcn_s_setprio(PNN_PRIO);
    unsigned aLo = lds0 + IDXP_OFF + ((16 * p + r8) * QP - 2) * 4;
    unsigned aHi = lds0 + IDXO_OFF + ((16 * p + r8) * OP + FB * h) * 4;
    unsigned aP = lds0 + IDXO_OFF + (16 * p * OP + r8) * 4;
    auto ldi = [&](unsigned a) { return *(LAS const int*)(size_t)a; };
    const unsigned ldsP = lds0 + P_OFF + p * P_WAVE;
    auto swz = [&](int row) { return (unsigned)(row * 128 + ((g4 ^ ((row >> 1) & 7)) << 4)); };
    unsigned aF0 = ldsP + frag0, aF1 = ldsP + frag1;
    unsigned aD0 = ldsP + (r16 < 12 ? frag0 : 12288 + swz(r16 - 12)), aD1 = aD0 + (r16 < 12 ? 2048 : 0);
    unsigned aX1 = ldsP + (r16 < 8 ? (r16 >> 2) * 2048 + swz(12 + (r16 & 3)) : 12288 + swz(r16 - 8));
    unsigned aX2 = ldsP + (2 + (r16 >> 2)) * 2048 + swz(12 + (r16 & 3));
    unsigned aD0k = aD0 ^ 64, aD1k = aD1 ^ 64, aX1k = aX1 ^ 64, aX2k = aX2 ^ 64;
    asm volatile("" : "+v"(aF0), "+v"(aF1), "+v"(aD0), "+v"(aD1), "+v"(aX1), "+v"(aX2), "+v"(aD0k), "+v"(aD1k), "+v"(aX1k), "+v"(aX2k));
    auto ldf = [&](unsigned a) { return *(LAS const u32x4*)(size_t)a; };
    const unsigned tailmask = r8 < 4 ? 0u : 0x80000000u;

    int ip[5];
#define LDA(J) ia[0] = ldi(aLo + 4 * (J)); ia[1] = ldi(aLo + 4 * (J) + 32 * QP); ia[2] = ldi(aHi + 4 * (J)); ia[3] = ldi(aHi + 4 * (J) + 32 * OP);
#define LDP(S, G) _Pragma("unroll") for (int i = 0; i < 4; ++i) ip[i] = ldi(aP + 4 * OP * (S) + 128 * G + 32 * i); \
                  if constexpr (G == 0) ip[4] = ldi(aP + 4 * OP * (S) + 384);
#define DP(G, I) { SB dma16((((unsigned)ip[I] << 7) + ((I & 1) ? chO : chE)) | PKILL, srdT, ldsP + G * 4096 + I * 1024); SB }
#define DT { SB dma16((((unsigned)ip[4] << 7) + chE) | tailmask | PKILL, srdT, ldsP + 12288); SB }
#define DP4(G) DP(G, 0) DP(G, 1) DP(G, 2) DP(G, 3)
    static_assert(MID == 8, "index tables");
    waitv<8>();
    TL(2)
    bar();
    const u32x2 tk = *(LAS const u32x2*)(lds + TOK_OFF + p * 8);
    const unsigned tlo = __builtin_amdgcn_readfirstlane(tk[0]), thi = __builtin_amdgcn_readfirstlane(tk[1]);
    f32x4 wp[23];
    {
      LAS const char* wl = lds + WPL_OFF;
      u32x4 R[6][4];
#pragma unroll
      for (int t = 0; t < 6; ++t)
#pragma unroll
        for (int kb = 0; kb < 4; ++kb) {
          LAS const char* rp = wl + (16 * t + r16) * 200 + 16 * g4 + 64 * kb;
          const u32x2 lo = *(LAS const u32x2*)rp, hi = *(LAS const u32x2*)(rp + 8);
          R[t][kb] = u32x4{lo[0], lo[1], hi[0], hi[1]};
        }
      auto one = [&](int k) {
        u32x4 v;
#pragma unroll
        for (int i = 0; i < 4; ++i) { const int d = k - 8 * g4 - 2 * i; v[i] = d == 0 ? 0x00003F80u : d == 1 ? 0x3F800000u : 0u; }
        return v;
      };
      auto w4 = [&](int row, int col0) {
        const u32x2 v = *(LAS const u32x2*)(wl + row * 200 + col0 * 2);
        return f32x4{__builtin_bit_cast(float, v[0] << 16), __builtin_bit_cast(float, v[0] & 0xffff0000u),
                     __builtin_bit_cast(float, v[1] << 16), __builtin_bit_cast(float, v[1] & 0xffff0000u)};
      };
      const f32x4 z = f32x4{0.f, 0.f, 0.f, 0.f};
      const u32x4 sel[2] = {one(r16), one(16 + r16)};
      const u32x4 selB[2] = {one(r16 < 12 ? r16 : -9), one(r16 < 12 ? 16 + r16 : -9)}, selT = one(r16 < 12 ? -9 : r16 - 12);
#pragma unroll
      for (int q = 1; q < 6; ++q)
#pragma unroll
        for (int u = 0; u < q; ++u) wp[q * (q - 1) / 2 + u] = MFMA(sel[u & 1], R[q][u >> 1], MFMA(R[u][q >> 1], sel[q & 1], z));
#pragma unroll
      for (int t = 0; t < 6; ++t) {
        const f32x4 d1 = MFMA(R[t][3], selT, MFMA(R[t][t >> 1], selB[t & 1], z));
        wp[15 + t] = w4(r16 < 12 ? 16 * t + r16 : 84 + r16, 16 * t + 4 * g4) + (g4 == 3 || r16 >= 12 ? d1 : z);
      }
      const bool same = g4 == (r16 >> 2);
      const int f1 = g4 < 2 ? 16 * g4 + 12 : 96, f2 = 16 * g4 + 44;
      wp[21] = same && g4 < 3 ? w4(f1 + (r16 & 3), f1) : z;
      wp[22] = same ? w4(f2 + (r16 & 3), f2) : z;
#pragma unroll
      for (int t = 0; t < 23; ++t) asm volatile("" : "+v"(wp[t]));
    }
    stage_idx();
    waitv<10>();
    TL(3)
    bar();
    TL(4)
    EA(2) DA4(2)
    EA(3) DA4(3)
    waitv<4>(); LDA(4) bar();
    static_assert(G0 >= 3, "start-up schedule");
#pragma unroll
    for (int n = 1; n < G0 - 2; ++n) { DA4(n + 3) LDA(n + 4) waitv<4>(); bar(); }
    LDP(0, 0) DA4(G0 + 1) DP4(0) DT LDA(G0 + 2) LDP(0, 1) waitv<9>();
    TL(5)
    bar();
    DA4(G0 + 2) DP4(1) LDA(G0 + 3) LDP(0, 2) waitv<8>(); bar();

    u32x4 fr[9][2];
#pragma unroll
    for (int t = 2; t < 6; ++t) { fr[t][0] = u32x4{0, 0, 0, 0}; fr[t][1] = u32x4{0, 0, 0, 0}; }
    float ps0 = 0.f, ps1 = 0.f, ps2 = 0.f, ps3 = 0.f;
    f32x4 gq[2][2];
    auto pm = [&](auto cc, auto u0c, auto q0c, auto u1c, auto q1c) {
      constexpr int c = decltype(cc)::value, u0 = decltype(u0c)::value, q0 = decltype(q0c)::value, u1 = decltype(u1c)::value, q1 = decltype(q1c)::value;
      const f32x4 z = f32x4{0.f, 0.f, 0.f, 0.f};
      SB
      __builtin_amdgcn_s_setprio(PM_PRIO);
      gq[c][0] = MFMA(fr[u0][0], fr[q0][0], z);
      if constexpr (u1 >= 0) gq[c][1] = MFMA(fr[u1][0], fr[q1][0], z);
      gq[c][0] = MFMA(fr[u0][1], fr[q0][1], gq[c][0]);
      if constexpr (u1 >= 0) gq[c][1] = MFMA(fr[u1][1], fr[q1][1], gq[c][1]);
      SB __builtin_amdgcn_s_setprio(PF_PRIO);
      SB
    };
    auto pf = [&](auto cc, auto w0c, auto w1c) {
      constexpr int c = decltype(cc)::value, i0 = decltype(w0c)::value, i1 = decltype(w1c)::value;
      const f32x4 w0 = wp[i0];
      ps0 = __builtin_fmaf(gq[c][0][0], w0[0], ps0); ps1 = __builtin_fmaf(gq[c][0][1], w0[1], ps1);
      ps2 = __builtin_fmaf(gq[c][0][2], w0[2], ps2); ps3 = __builtin_fmaf(gq[c][0][3], w0[3], ps3);
      if constexpr (i1 >= 0) {
        const f32x4 w1v = wp[i1];
        ps0 = __builtin_fmaf(gq[c][1][0], w1v[0], ps0); ps1 = __builtin_fmaf(gq[c][1][1], w1v[1], ps1);
        ps2 = __builtin_fmaf(gq[c][1][2], w1v[2], ps2); ps3 = __builtin_fmaf(gq[c][1][3], w1v[3], ps3);
      }
      SB
    };
#define PM(C, U0, Q0, U1, Q1) pm(IC(C), IC(U0), IC(Q0), IC(U1), IC(Q1));
#define PF(C, I0, I1) pf(IC(C), IC(I0), IC(I1));
#define WO(U, Q) (Q * (Q - 1) / 2 + U)
    float pv;
    auto total0 = [&]() {
      asm("" : "+v"(ps0)); asm("" : "+v"(ps1)); asm("" : "+v"(ps2)); asm("" : "+v"(ps3));
      pv = (ps0 + ps1) + (ps2 + ps3);
      asm volatile("" : "+v"(pv));
      ps0 = ps1 = ps2 = ps3 = 0.f;
    };
    auto total1 = [&](int s) {
      float tot = wave_sum63(pv);
      if (lane == 63 && s >= 0) red[256 + 16 * p + s] = tot;
    };
#define RDF(D, T) fr[D][0] = ldf(aF0 + T * 2048); fr[D][1] = ldf(aF1 + T * 2048);
#define RDX(D, A) fr[D][0] = ldf(A); fr[D][1] = ldf(A##k);
#define RDD(T) if (r16 < 12) { fr[6][0] = ldf(aD0 + T * 2048); fr[6][1] = ldf(aD0k + T * 2048); \
                              fr[7][0] = ldf(aD0 + (T + 1) * 2048); fr[7][1] = ldf(aD0k + (T + 1) * 2048); }
    auto xpoll = [&]() {
      GAS u64* pf = (GAS u64*)(ws + XFLAG_OFF + (bid ^ 8) * 256 + p * 64);
      const u64 tok = ((u64)thi << 32) | tlo;
      for (unsigned spins = 0; spins < (1u << 18); ++spins) {
        if (__all(__hip_atomic_load(pf, RLX_AGENT) == tok)) break;
        __builtin_amdgcn_s_sleep(1);
      }
      asm volatile("" ::: "memory");
    };
    auto steps = [&](int s, auto fac, auto nbarc, auto lastc) {
      constexpr bool fa = decltype(fac)::value, last = decltype(lastc)::value;
      constexpr int nbar = decltype(nbarc)::value;
      const int n = 3 * s + G0;
#define XA(J, I) if constexpr (fa) DA(J, I)
#define XP(G, I) if constexpr (!last) DP(G, I)
#define ENDSTEP(CNT, K) { float x0 = ps0, x1 = ps1, x2 = ps2, x3 = ps3; asm volatile("" : "+v"(x0), "+v"(x1), "+v"(x2), "+v"(x3)); ps0 = x0; ps1 = x1; ps2 = x2; ps3 = x3; } \
      STAMP(1) waitv<CNT>(); STAMP(2) if constexpr (nbar == 2 && K == 1) xpoll(); if constexpr (nbar > K) bar(); else { SB asm volatile("s_waitcnt lgkmcnt(0)" ::: "memory"); SB } STAMP(3)
      asm volatile("" : "+v"(aLo), "+v"(aHi), "+v"(aP));
      RDF(0, 0) RDF(1, 1) RDX(6, aD0) RDX(7, aD1) RDX(8, aX1)
      PM(0, 4, 5, 3, 5) XA(n + 3, 0) DP(2, 0)
      PM(1, 3, 4, 2, 5) PF(0, WO(4, 5), WO(3, 5)) XA(n + 3, 1) DP(2, 1)
      PM(0, 0, 1, 0, 6) PF(1, WO(3, 4), WO(2, 5)) total0(); if constexpr (!TOTB) total1(s - 1); XA(n + 3, 2) DP(2, 2)
      PM(1, 1, 7, 8, 8) PF(0, WO(0, 1), 15) XA(n + 3, 3) DP(2, 3) PF(1, 16, 21)
      if constexpr (fa) { LDA(G0 + 4) }
      if constexpr (!last) { LDP(1, 0) }
      ENDSTEP((fa ? 8 : 4), 0)
      RDF(2, 2) RDF(3, 3) RDD(2)
      XA(n + 4, 0) XP(0, 0)
      if constexpr (TOTB) total1(s - 1);
      PM(0, 0, 2, 1, 2) XA(n + 4, 1) XP(0, 1)
      PM(1, 0, 3, 1, 3) PF(0, WO(0, 2), WO(1, 2)) XA(n + 4, 2) XP(0, 2)
      PM(0, 2, 3, 2, 6) PF(1, WO(0, 3), WO(1, 3)) XA(n + 4, 3) XP(0, 3)
      PM(1, 3, 7, -1, -1) PF(0, WO(2, 3), 17)
      if constexpr (!last) DT
      PF(1, 18, -1)
      if constexpr (fa) { LDA(G0 + 5) }
      if constexpr (!last) { LDP(1, 1) }
      ENDSTEP(((fa ? 4 : 0) + (last ? 0 : 5)), 1)
      RDX(8, aX2) RDF(4, 4) RDF(5, 5) RDD(4)
      XA(n + 5, 0)
      PM(0, 8, 8, 0, 4) XP(1, 0) XA(n + 5, 1)
      PM(1, 1, 4, 2, 4) PF(0, 22, WO(0, 4)) XP(1, 1) XA(n + 5, 2)
      PM(0, 0, 5, 1, 5) PF(1, WO(1, 4), WO(2, 4)) XP(1, 2) XA(n + 5, 3)
      PM(1, 4, 6, 5, 7) PF(0, WO(0, 5), WO(1, 5)) XP(1, 3) PF(1, 19, 20)
      if constexpr (fa) { LDA(G0 + 6) }
      if constexpr (!last) { LDP(1, 2) }
      aLo += 12; aHi += 12; aP += 4 * OP;
      ENDSTEP(((fa ? 4 : 0) + (last ? 0 : 4)), 2)
    };
    for (int s = 0; s < S2 - 1; ++s) steps(s, T{}, IC(3), Fa{});
    if constexpr (POST == 0) steps(15, Fa{}, IC(2), T{});
    else {
      steps(S2 - 1, Fa{}, IC(2), Fa{});
      for (int s = S2; s < 15; ++s) steps(s, Fa{}, IC(0), Fa{});
      steps(15, Fa{}, IC(0), T{});
    }
    PM(0, 4, 5, 3, 5) PM(1, 3, 4, 2, 5) PF(0, WO(4, 5), WO(3, 5)) PF(1, WO(3, 4), WO(2, 5))
    total0(); total1(15);
    if (bid == 0) {
      sweep2(rsW, WFLAG_OFF + p * 2048 + lane * 16, tlo, thi);
      sweep2(rsW, WFLAG_OFF + p * 2048 + 1024 + lane * 16, tlo, thi);
      if (lane == 0) __hip_atomic_store((GAS u64*)(ws + EPOCH_OFF + p * 256), ((u64)thi << 32) | tlo, RLX_AGENT);
    }
  } else {
    LAS float* bw = (LAS float*)(lds + BW_OFF);
    const i32x4 srdW = make_srd(ws, W1T_BYTES);
    const unsigned voffW = lane * 16;
    const unsigned wbase = h * (FB * 32768) + wid * 8192;
    WSet w0, w1s, w2s, w3;
#define W_LD4(S, OFF) w_ld<0>(S, voffW, srdW, OFF); w_ld<1>(S, voffW, srdW, OFF); w_ld<2>(S, voffW, srdW, OFF); w_ld<3>(S, voffW, srdW, OFF);
    f32x4 acc[8][4];
    u64 tok;
    {
      n_wait<4 + PTOUCH>(raw);
      asm volatile("" : "+v"(ep));
      TL(1)
      WSet o; n_frag(raw, o);
      const unsigned dst = bid < 200 ? (bid >> 1) * 32768 + wid * 8192 + (bid & 1) * 4096 + lane * 16 : 0x80000000u;
#pragma unroll
      for (int c = 0; c < 4; ++c) __builtin_amdgcn_raw_buffer_store_b128(o.r[c], rsW, dst + c * 1024, 0, 16);
      const u64 e = ((u64)(unsigned)__builtin_amdgcn_readfirstlane((int)ep[1]) << 32) | (unsigned)__builtin_amdgcn_readfirstlane((int)ep[0]);
      tok = (e ^ 0x9E3779B97F4A7C15ull) * 0xBF58476D1CE4E5B9ull + 0x632BE59BD9B4E019ull;
      if (tok == e) tok = ~e;
      asm volatile("s_waitcnt vmcnt(0)" : "+v"(bq0), "+v"(bq1), "+v"(w2v), "+v"(b2v), "+v"(pt) :: "memory");
      TL(2)
      if (lane == 0) {
        __hip_atomic_store((GAS u64*)(ws + WFLAG_OFF + wid * 2048 + bid * 8), tok, RLX_AGENT);
        *(LAS u64*)(lds + TOK_OFF + wid * 8) = tok;
      }
      bw[wid * 64 + lane] = __builtin_bit_cast(float, w2v << 16);
      if (tid == 0) bw[256] = __builtin_bit_cast(float, b2v << 16);
#pragma unroll
      for (int m = 0; m < 8; ++m)
#pragma unroll
        for (int nn = 0; nn < 4; ++nn) {
#pragma unroll
          for (int r = 0; r < 4; ++r) {
            const unsigned d = (r < 2 ? bq0 : bq1)[(r & 1) * 2 + (nn >> 1)];
            acc[m][nn][r] = m < 4 ? 0.f : __builtin_bit_cast(float, (nn & 1) ? (d & 0xffff0000u) : (d << 16));
          }
          asm volatile("" : "+v"(acc[m][nn]));
        }
      bar();
      waitv<0>();
      __builtin_amdgcn_s_sleep(PDELAY);
      TL(3)
      sweep2(rsW, WFLAG_OFF + wid * 2048 + h * (2 * MAINS * 8) + (lane < FB ? lane : FB - 1) * 16, (unsigned)tok, (unsigned)(tok >> 32));
      TL(4)
    }
    W_LD4(w0, wbase) W_LD4(w1s, wbase + 4096) W_LD4(w2s, wbase + 32768)
    TL(5)
    bar();
    TL(6)
    u32x4 a[4];
    unsigned b0 = frag0, b1a = frag1;
#pragma unroll
    for (int m = 0; m < 3; ++m) a[m] = *(LAS u32x4*)(lds + b0 + m * 2048);
#define GRP(CUR, M, RD, LD) \
      mfma1(acc[M][0], CUR.r[0], a[M & 3]); SB RD SB \
      mfma1(acc[M][1], CUR.r[1], a[M & 3]); LD \
      mfma1(acc[M][2], CUR.r[2], a[M & 3]); touch(a[(M + 1) & 3]); \
      mfma1(acc[M][3], CUR.r[3], a[M & 3]); SB
#define RDK(M) if constexpr (M < 5) a[(M + 3) & 3] = *(LAS u32x4*)(lds + cb + co + (M + 3) * 2048); \
               else a[(M + 3) & 3] = *(LAS u32x4*)(lds + nb + no + (M - 5) * 2048);
    auto kstep = [&](WSet& cur, WSet& nxt, unsigned soff, unsigned cb, auto coc, unsigned nb, auto noc) {
      constexpr int co = decltype(coc)::value, no = decltype(noc)::value;
      w_wait<8>(cur);
      SB
#define G(M) GRP(cur, M, RDK(M), if constexpr ((M & 1) == 0) w_ld<M / 2>(nxt, voffW, srdW, soff);)
      G(0) G(1) G(2) G(3) G(4) G(5) G(6) G(7)
#undef G
    };
    unsigned so = wbase + 32768 + 4096;
    const unsigned wmid = MAINS * 32768 + wid * 8192;
    for (int n = 0; n < MAINS; n += 2) {
      kstep(w0, w3, so, b0, IC(0), b1a, IC(0));
      if (n == MAINS - 2) so = wmid + t_woff(0) - 28672;
      kstep(w1s, w0, so + 28672, b1a, IC(0), b0, IC(A_STAGE));
      STAMP(0) bar_nw(); STAMP(3)
      kstep(w2s, w1s, so + 32768, b0, IC(A_STAGE), b1a, IC(A_STAGE));
      b0 ^= 2 * A_STAGE;
      if (n == MAINS - 2) so = wmid + t_woff(2) - 32768 - 28672;
      kstep(w3, w2s, so + 32768 + 28672, b1a, IC(A_STAGE), b0, IC(0));
      b1a ^= 2 * A_STAGE;
      STAMP(0) bar_nw(); STAMP(3)
      so += 65536;
    }
    u32x4 px[8];
    {
      const i32x4 srdS = make_srd(ws + SLAB_OFF + (size_t)bid * SLAB_BYTES, SLAB_BYTES);
      const i32x4 srdR = make_srd(ws + SLAB_OFF + (size_t)(bid ^ 8) * SLAB_BYTES, SLAB_BYTES);
      WSet ts[NU];
      ts[0] = w0; ts[1] = w1s; ts[2] = w2s;
      auto top = [&](auto uc, auto gc) {
        constexpr int u = decltype(uc)::value, g = decltype(gc)::value, st = t_store(u, g), ld = t_load(u, g), x = t_w0(u) + t_wn(u) * g;
        if constexpr (st >= 0) {
          const f32x4 lo = acc[st >> 1][2 * (st & 1)], hi = acc[st >> 1][2 * (st & 1) + 1];
          x_st<st & 3>(u32x4{pk_bf16(lo[0], lo[1]), pk_bf16(lo[2], lo[3]), pk_bf16(hi[0], hi[1]), pk_bf16(hi[2], hi[3])}, voffW, srdS, wid * 8192 + (st >> 2) * 4096);
        }
        if constexpr (ld >= 0) x_ld<ld & 3>(px[ld], voffW, srdR, wid * 8192 + (ld >> 2) * 4096);
        if constexpr (t_wn(u) > 0) w_ld<x & 3>(ts[x >> 2], voffW, srdW, wmid + t_woff(x >> 2));
        if constexpr (t_wn(u) > 1) w_ld<(x + 1) & 3>(ts[x >> 2], voffW, srdW, wmid + t_woff(x >> 2));
      };
      auto hk = [&](auto uc) {
        constexpr int U = decltype(uc)::value;
        constexpr int co = ((MAINS + U / 4) & 3) * A_STAGE + ((U >> 1) & 1) * 8192;
        constexpr int no = ((MAINS + (U + 1) / 4) & 3) * A_STAGE + (((U + 1) >> 1) & 1) * 8192;
        const unsigned cb = (U & 1) ? frag1 : frag0, nb = (U & 1) ? frag0 : frag1;
        WSet& cur = ts[U];
        w_wait<tail_wait(U)>(cur);
        if constexpr (U == FLAG_U) {
          if (lane == 0) __hip_atomic_store((GAS u64*)(ws + XFLAG_OFF + bid * 256 + wid * 64), tok, RLX_AGENT);
        }
        SB
#define RDR(M) if constexpr (M == 0) a[3] = *(LAS u32x4*)(lds + cb + co + 3 * 2048); \
               else if constexpr (U + 1 < NU) a[M - 1] = *(LAS u32x4*)(lds + nb + no + (M - 1) * 2048);
        GRP(cur, 4, RDR(0), top(uc, IC(0));) GRP(cur, 5, RDR(1), top(uc, IC(1));) GRP(cur, 6, RDR(2), top(uc, IC(2));) GRP(cur, 7, RDR(3), top(uc, IC(3));)
      };
#define HK4(J) hk(IC(4 * J)); hk(IC(4 * J + 1)); hk(IC(4 * J + 2)); hk(IC(4 * J + 3));
      HK4(0) bar_nw(); HK4(1) bar_nw(); HK4(2) bar_nw();
      HK4((MID / 2 - 1))
      asm volatile("s_nop 7"); SB
    }
    __builtin_amdgcn_s_setprio(2);
    f32x4 ww[4];
#pragma unroll
    for (int r = 0; r < 4; ++r) ww[r] = *(LAS f32x4*)(bw + wid * 64 + g4 * 16 + r * 4);
    f32x4 wt[4];
#pragma unroll
    for (int nn = 0; nn < 4; ++nn) wt[nn] = f32x4{ww[0][nn], ww[1][nn], ww[2][nn], ww[3][nn]};
#pragma unroll
    for (int m = 0; m < 4; ++m) {
      float s = 0.f;
      if (m == 0) x_wait<6>(px[0], px[1]); else if (m == 1) x_wait<4>(px[2], px[3]); else if (m == 2) x_wait<2>(px[4], px[5]); else x_wait<0>(px[6], px[7]);
      f32x2 s2 = f32x2{0.f, 0.f};
#pragma unroll
      for (int nn = 0; nn < 4; ++nn)
#pragma unroll
        for (int r = 0; r < 4; r += 2) {
          const unsigned pd = px[2 * m + (nn >> 1)][2 * (nn & 1) + (r >> 1)];
          const f32x2 x = f32x2{acc[4 + m][nn][r], acc[4 + m][nn][r + 1]} + f32x2{__builtin_bit_cast(float, pd << 16), __builtin_bit_cast(float, pd & 0xffff0000u)};
          const f32x2 t = __builtin_elementwise_fma(x * x, f32x2{-0.1029432397f, -0.1029432397f}, f32x2{-2.302208198f, -2.302208198f});
          const f32x2 z = x * t;
          const f32x2 d = f32x2{__builtin_amdgcn_exp2f(z[0]), __builtin_amdgcn_exp2f(z[1])} + f32x2{1.f, 1.f};
          const f32x2 y = x * f32x2{wt[nn][r], wt[nn][r + 1]};
          s2 = __builtin_elementwise_fma(y, f32x2{__builtin_amdgcn_rcpf(d[0]), __builtin_amdgcn_rcpf(d[1])}, s2);
        }
      s = s2[0] + s2[1];
      s += __shfl_xor(s, 16);
      s += __shfl_xor(s, 32);
      if (lane < 16) red[wid * 64 + m * 16 + lane] = s;
    }
  }
  bar();
  if (tid < 64) {
    float v = red[256 + tid] + ((red[tid] + red[64 + tid]) + (red[128 + tid] + red[192 + tid])) + *(LAS const float*)(lds + BW_OFF + 1024);
    out[sown + tid] = __float2bfloat16(v);
  }
}

extern "C" void kernel_launch(void* const* d_in, const int* in_sizes, int n_in, void* d_out, int out_size, void* d_ws, size_t ws_size,
                              hipStream_t stream) {
  (void)hipFuncSetAttribute((const void*)pnn_kernel, hipFuncAttributeMaxDynamicSharedMemorySize, LDS_BYTES);
  pnn_kernel<<<NWG, 512, LDS_BYTES, stream>>>((const int*)d_in[0], (const char*)d_in[1], (u64)in_sizes[1] * 2ull, (const char*)d_in[2],
                                              (const char*)d_in[3], (const unsigned short*)d_in[4], (const unsigned short*)d_in[5],
                                              (const unsigned short*)d_in[6], (char*)d_ws, (__hip_bfloat16*)d_out);
}
```

```cpp
#include <hip/hip_runtime.h>
#include <hip/hip_bf16.h>
#include <stdint.h>
#include <type_traits>

typedef float f32x4 __attribute__((ext_vector_type(4)));
typedef float f32x2 __attribute__((ext_vector_type(2)));
typedef int i32x4 __attribute__((ext_vector_type(4)));
typedef int i32x2 __attribute__((ext_vector_type(2)));
typedef unsigned u32x4 __attribute__((ext_vector_type(4)));
typedef unsigned u32x2 __attribute__((ext_vector_type(2)));
typedef __bf16 bf16x8 __attribute__((ext_vector_type(8)));
typedef unsigned long long u64;
#define LAS __attribute__((address_space(3)))
#define GAS __attribute__((address_space(1)))

constexpr int NB = 16384, NF = 100, ND = 64, NH = 256, HF = 50;
constexpr int NWG = 256;
#define POST 1
#define PHOLD 0
#define PKG 1
#define PSLEEP 8
#define PDELAY 12
#define HIDEF 0
#define TOTB 1
#define STG 3
#define PKILL 0u
#define PM_PRIO 1
#define PF_PRIO 0
#define PNN_PRIO 1
#define MID 8
constexpr int MAINS = (NF - MID) / 2, FB = MAINS + MID, NU = 2 * MID;
constexpr int S2 = 16 - POST;
constexpr int G0 = HF - 3 * S2;
static_assert(G0 >= 2 && MAINS % 2 == 0 && MAINS <= 48, "schedule");
constexpr int FLAG_U = NU - 8;
constexpr int t_store(int u, int g) {
  if (MID >= 12) return g == 1 && u < 8 ? u : -1;
  return u < 3 ? ((g & 1) ? 2 * u + (g >> 1) : -1) : (u < 5 && g == 1 ? u + 3 : -1);
}
constexpr int t_load(int u, int g) { return u >= NU - 4 && (g & 1) ? 2 * (u - NU + 4) + (g >> 1) : -1; }
constexpr int t_iss(int v) {
  if (v < 3) return -1;
  if (v <= FLAG_U + 3) { const int x = FLAG_U - 2 - (FLAG_U + 3 - v) / 2; return v - 3 < x ? v - 3 : x; }
  return FLAG_U + (v - FLAG_U - 4) / 2;
}
constexpr int t_woff(int v) { return ((v >> 2) + ((v & 2) ? 0 : 4)) * 32768 + (v & 1) * 4096; }
constexpr int t_wn(int u) { int c = 0; for (int v = 3; v < NU; ++v) c += t_iss(v) == u; return c; }
constexpr int t_w0(int u) { for (int v = 3; v < NU; ++v) if (t_iss(v) == u) return 4 * v; return 0; }
constexpr int tail_wait(int u) {
  if (u == FLAG_U) return 0;
  int cnt = u < 3 ? 4 * (2 - u) : 0;
  for (int v = 0; v < u; ++v) {
    if (v == FLAG_U) ++cnt;
    for (int g = 0; g < 4; ++g) {
      cnt += (t_store(v, g) >= 0) + (t_load(v, g) >= 0);
      if (t_wn(v) > 0 && t_w0(v) + t_wn(v) * (g + 1) - 1 == 4 * u + 3) cnt = 0; else cnt += t_wn(v);
    }
  }
  return cnt;
}
constexpr int W1T_BYTES = NF * ND * NH * 2;
constexpr int EPOCH_OFF = W1T_BYTES;
constexpr int WFLAG_OFF = EPOCH_OFF + 1024;
constexpr int XFLAG_OFF = WFLAG_OFF + 8192;
constexpr int SLAB_OFF = XFLAG_OFF + NWG * 256;
constexpr int SLAB_BYTES = 32768;
constexpr int A_STAGE = 16384;
constexpr int P_OFF = 4 * A_STAGE;
constexpr int P_WAVE = 13 * 1024;
constexpr int WPL_OFF = 2 * A_STAGE;
constexpr int OP = 104;
constexpr int IDXO_OFF = P_OFF + 4 * P_WAVE;
constexpr int IDXP_OFF = IDXO_OFF + 64 * OP * 4;
constexpr int QP = 48;
constexpr int RED_OFF = IDXP_OFF + 64 * QP * 4;
constexpr int BW_OFF = RED_OFF + 5 * 64 * 4;
constexpr int TOK_OFF = BW_OFF + 260 * 4;
constexpr int LDS_BYTES = TOK_OFF + 32;
static_assert(LDS_BYTES <= 160 * 1024, "LDS");

__device__ __forceinline__ float bf2f(unsigned short h) { return __builtin_bit_cast(float, (unsigned)h << 16); }
__device__ __forceinline__ void dma16(unsigned voff, i32x4 srd, unsigned lds_dst) {
  asm volatile("s_mov_b32 m0, %0\n\ts_nop 0\n\tbuffer_load_dwordx4 %1, %2, 0 offen lds" :: "s"(lds_dst), "v"(voff), "s"(srd) : "memory");
}
template <int N> __device__ __forceinline__ void waitv() { asm volatile("s_waitcnt vmcnt(%0)" :: "i"(N) : "memory"); }
#define SB __builtin_amdgcn_sched_barrier(0);
__device__ __forceinline__ void bar() {
  SB asm volatile("s_waitcnt lgkmcnt(0)" ::: "memory");
  __builtin_amdgcn_s_barrier();
  asm volatile("" ::: "memory"); SB
}
__device__ __forceinline__ void bar_nw() {
  SB __builtin_amdgcn_s_barrier();
  asm volatile("" ::: "memory"); SB
}
__device__ __forceinline__ i32x4 make_srd(const void* p, unsigned bytes) {
  u64 a = (u64)p;
  i32x4 r;
  r[0] = __builtin_amdgcn_readfirstlane((int)(unsigned)a);
  r[1] = __builtin_amdgcn_readfirstlane((int)(unsigned)(a >> 32) & 0xffff);
  r[2] = (int)bytes;
  r[3] = 0x00020000;
  return r;
}
#define MFMA(a, b, c) __builtin_amdgcn_mfma_f32_16x16x32_bf16(__builtin_bit_cast(bf16x8, a), __builtin_bit_cast(bf16x8, b), c, 0, 0, 0)
#define IC(x) std::integral_constant<int, x>{}
#define RLX_AGENT __ATOMIC_RELAXED, __HIP_MEMORY_SCOPE_AGENT

struct WSet { u32x4 r[4]; };
template <int J> __device__ __forceinline__ void w_ld(WSet& s, unsigned voff, i32x4 srd, unsigned soff) {
  asm volatile("buffer_load_dwordx4 %0, %1, %2, %3 offen offset:%4 sc1" : "=v"(s.r[J]) : "v"(voff), "s"(srd), "s"(soff), "i"(J * 1024) : "memory");
}
template <int N> __device__ __forceinline__ void w_wait(WSet& s) {
  asm volatile("s_waitcnt vmcnt(%4)" : "+v"(s.r[0]), "+v"(s.r[1]), "+v"(s.r[2]), "+v"(s.r[3]) : "i"(N) : "memory");
}
template <int J> __device__ __forceinline__ void x_st(const u32x4& v, unsigned voff, i32x4 srd, unsigned soff) {
  asm volatile("buffer_store_dwordx4 %0, %1, %2, %3 offen offset:%4 sc1\n\ts_nop 1" :: "v"(v), "v"(voff), "s"(srd), "s"(soff), "i"(J * 1024) : "memory");
}
template <int J> __device__ __forceinline__ void x_ld(u32x4& v, unsigned voff, i32x4 srd, unsigned soff) {
  asm volatile("buffer_load_dwordx4 %0, %1, %2, %3 offen offset:%4 sc1" : "=v"(v) : "v"(voff), "s"(srd), "s"(soff), "i"(J * 1024) : "memory");
}
template <int N> __device__ __forceinline__ void x_wait(u32x4& c0, u32x4& c1) {
  asm volatile("s_waitcnt vmcnt(%2)" : "+v"(c0), "+v"(c1) : "i"(N) : "memory");
}
__device__ __forceinline__ unsigned pk_bf16(float lo, float hi) {
  typedef __bf16 bf16x2 __attribute__((ext_vector_type(2)));
  return __builtin_bit_cast(unsigned, bf16x2{(__bf16)lo, (__bf16)hi});
}
__device__ __forceinline__ void mfma1(f32x4& c, const u32x4& w, const u32x4& e) {
  asm volatile("v_mfma_f32_16x16x32_bf16 %0, %1, %2, %0" : "+v"(c) : "v"(w), "v"(e));
}
__device__ __forceinline__ void touch(const u32x4& x) { asm volatile("" :: "v"(x)); }
struct NSet { u32x2 r[8]; };
template <int J> __device__ __forceinline__ void n_ld(NSet& s, unsigned voff, i32x4 srd, unsigned soff) {
  asm volatile("buffer_load_dwordx2 %0, %1, %2, %3 offen offset:%4" : "=v"(s.r[J]) : "v"(voff), "s"(srd), "s"(soff), "i"(J * 512) : "memory");
}
template <int N> __device__ __forceinline__ void n_wait(NSet& s) {
  asm volatile("s_waitcnt vmcnt(%8)"
               : "+v"(s.r[0]), "+v"(s.r[1]), "+v"(s.r[2]), "+v"(s.r[3]), "+v"(s.r[4]), "+v"(s.r[5]), "+v"(s.r[6]), "+v"(s.r[7]) : "i"(N) : "memory");
}
__device__ __forceinline__ void n_frag(const NSet& s, WSet& o) {
#pragma unroll
  for (int c = 0; c < 4; ++c)
#pragma unroll
    for (int i = 0; i < 4; ++i) o.r[c][i] = __builtin_amdgcn_perm(s.r[2 * i + 1][c >> 1], s.r[2 * i][c >> 1], (c & 1) ? 0x07060302u : 0x05040100u);
}
#define N_LD8(S, V, OFF) n_ld<0>(S, V, srdN, OFF); n_ld<1>(S, V, srdN, OFF); n_ld<2>(S, V, srdN, OFF); n_ld<3>(S, V, srdN, OFF); \
                         n_ld<4>(S, V, srdN, OFF); n_ld<5>(S, V, srdN, OFF); n_ld<6>(S, V, srdN, OFF); n_ld<7>(S, V, srdN, OFF);
template <int CTRL, int RM> __device__ __forceinline__ float dpp_add(float v) {
  return v + __builtin_bit_cast(float, __builtin_amdgcn_update_dpp(0, __builtin_bit_cast(int, v), CTRL, RM, 0xf, true));
}
__device__ __forceinline__ float wave_sum63(float v) {
  v = dpp_add<0x111, 0xf>(v); v = dpp_add<0x112, 0xf>(v); v = dpp_add<0x114, 0xf>(v); v = dpp_add<0x118, 0xf>(v);
  v = dpp_add<0x142, 0xa>(v); v = dpp_add<0x143, 0xc>(v);
  return v;
}
__device__ __forceinline__ void sweep2(__amdgpu_buffer_rsrc_t rs, unsigned off, unsigned tlo, unsigned thi) {
  for (unsigned spins = 0; spins < (1u << 18); ++spins) {
    asm volatile("" ::: "memory");
    const u32x4 v = __builtin_amdgcn_raw_buffer_load_b128(rs, off, 0, 16);
    if (__all(v[0] == tlo && v[1] == thi && v[2] == tlo && v[3] == thi)) break;
    __builtin_amdgcn_s_sleep(PSLEEP);
  }
  asm volatile("" ::: "memory");
}
#define TL(i)
#define STAMP(i)

__global__ __launch_bounds__(512, 2) void pnn_kernel(const int* __restrict__ idx, const char* __restrict__ table, u64 table_bytes,
                                                     const char* __restrict__ wpg, const char* __restrict__ w1,
                                                     const unsigned short* __restrict__ b1, const unsigned short* __restrict__ w2,
                                                     const unsigned short* __restrict__ b2, char* __restrict__ ws,
                                                     __hip_bfloat16* __restrict__ out) {
  extern __shared__ __attribute__((aligned(16))) char lds_raw[];
  LAS char* lds = (LAS char*)lds_raw;
  const int tid = threadIdx.x, lane = tid & 63, wid = __builtin_amdgcn_readfirstlane(tid >> 6);
  const int bid = blockIdx.x, h = (bid >> 3) & 1;
  const int pair = (bid & 7) * 16 + (bid >> 4);
  const int sown = pair * 128 + 64 * h, spar = pair * 128 + 64 * (h ^ 1);
  const unsigned lds0 = (unsigned)(size_t)lds;
  const int r16 = lane & 15, g4 = lane >> 4;
  const unsigned frag0 = r16 * 128 + ((g4 ^ ((r16 >> 1) & 7)) << 4);
  const unsigned frag1 = frag0 ^ 64;
  LAS float* red = (LAS float*)(lds + RED_OFF);
  using T = std::true_type; using Fa = std::false_type;
  const __amdgpu_buffer_rsrc_t rsW = __builtin_amdgcn_make_buffer_rsrc(ws, 0, SLAB_OFF, 0x00020000);
  const i32x4 srdN = make_srd(w1, W1T_BYTES);

  const int p = wid - 4, r8 = lane >> 3;
  const i32x4 srdI = make_srd(idx, NB * NF * 4);
  const i32x4 srdT = make_srd(table, (unsigned)table_bytes);
  const unsigned chE = (unsigned)(((lane & 7) ^ (lane >> 4)) << 4);
  const unsigned chO = chE ^ 64;
  const unsigned ldsA = lds0 + p * 2048;
  int ia[4];
#define DA(J, I) { SB dma16((((unsigned)ia[I] << 7) + ((I & 1) ? chO : chE)), srdT, ldsA + ((J) & 3) * A_STAGE + (I >> 1) * 8192 + (I & 1) * 1024); SB }
#define DA4(J) DA(J, 0) DA(J, 1) DA(J, 2) DA(J, 3)
#define EA(J) ia[0] = e0[J]; ia[1] = e1[J]; ia[2] = e2[J]; ia[3] = e3[J];
  auto stage_idx = [&]() {
    const unsigned oo = (unsigned)(sown + 16 * p) * 400u, op = (unsigned)(spar + 16 * p) * 400u + (FB * h + 2) * 4u;
#pragma unroll
    for (int i = 0; i < 3; ++i) {
      const unsigned c = lane + 64 * i, row = c / 12, k = c % 12;
      dma16(row * 400 + (k < 11 ? op + k * 16 : oo + (MAINS + 4) * 4), srdI, lds0 + IDXP_OFF + p * (16 * QP * 4) + i * 1024);
    }
#pragma unroll
    for (int i = 0; i < 7; ++i) {
      const unsigned c = lane + 64 * i, row = c / 26, k = c % 26;
      if (i < 6 || lane < 32) dma16(oo + row * 400 + (k < 25 ? k * 16 : MAINS * 4), srdI, lds0 + IDXO_OFF + p * (16 * OP * 4) + i * 1024);
    }
  };
  NSet raw; u32x2 ep; u32x4 bq0, bq1; unsigned w2v, b2v;
  i32x4 e0, e1, e2, e3;
  if (wid < 4) {
    N_LD8(raw, g4 * 4096 + wid * 128 + r16 * 8, (unsigned)(bid < 200 ? bid : 0) * 16384u)
    asm volatile("global_load_dwordx2 %0, %1, off sc1" : "=v"(ep) : "v"(ws + EPOCH_OFF + wid * 256) : "memory");
    asm volatile("global_load_dwordx4 %0, %1, off" : "=v"(bq0) : "v"(b1 + wid * 64 + g4 * 16) : "memory");
    asm volatile("global_load_dwordx4 %0, %1, off offset:16" : "=v"(bq1) : "v"(b1 + wid * 64 + g4 * 16) : "memory");
    asm volatile("global_load_ushort %0, %1, off" : "=v"(w2v) : "v"(w2 + wid * 64 + lane) : "memory");
    asm volatile("global_load_ushort %0, %1, off" : "=v"(b2v) : "v"(b2) : "memory");
  } else {
    const unsigned eq = (unsigned)(spar + 16 * p + r8) * 400u + FB * 4u * h, eo = (unsigned)(sown + 16 * p + r8) * 400u + FB * 4u * h;
    asm volatile("buffer_load_dwordx4 %0, %1, %2, 0 offen" : "=v"(e0) : "v"(eq), "s"(srdI) : "memory");
    asm volatile("buffer_load_dwordx4 %0, %1, %2, 0 offen offset:3200" : "=v"(e1) : "v"(eq), "s"(srdI) : "memory");
    asm volatile("buffer_load_dwordx4 %0, %1, %2, 0 offen" : "=v"(e2) : "v"(eo), "s"(srdI) : "memory");
    asm volatile("buffer_load_dwordx4 %0, %1, %2, 0 offen offset:3200" : "=v"(e3) : "v"(eo), "s"(srdI) : "memory");
    const i32x4 srdP = make_srd(wpg, NF * NF * 2);
#pragma unroll
    for (int i = 0; i < 5; ++i) dma16(p * 5120 + i * 1024 + lane * 16, srdP, lds0 + WPL_OFF + p * 5120 + i * 1024);
    asm volatile("s_waitcnt vmcnt(5)" : "+v"(e0), "+v"(e1), "+v"(e2), "+v"(e3) :: "memory");
    TL(1)
    EA(0) DA4(0) EA(1) DA4(1)
  }
  int role = wid;
  asm volatile("" : "+s"(role));
  if (role >= 4) {
    __builtin_amdgcn_s_setprio(PNN_PRIO);
    unsigned aLo = lds0 + IDXP_OFF + ((16 * p + r8) * QP - 2) * 4;
    unsigned aHi = lds0 + IDXO_OFF + ((16 * p + r8) * OP + FB * h) * 4;
    unsigned aP = lds0 + IDXO_OFF + (16 * p * OP + r8) * 4;
    auto ldi = [&](unsigned a) { return *(LAS const int*)(size_t)a; };
    const unsigned ldsP = lds0 + P_OFF + p * P_WAVE;
    auto swz = [&](int row) { return (unsigned)(row * 128 + ((g4 ^ ((row >> 1) & 7)) << 4)); };
    unsigned aF0 = ldsP + frag0, aF1 = ldsP + frag1;
    unsigned aD0 = ldsP + (r16 < 12 ? frag0 : 12288 + swz(r16 - 12)), aD1 = aD0 + (r16 < 12 ? 2048 : 0);
    unsigned aX1 = ldsP + (r16 < 8 ? (r16 >> 2) * 2048 + swz(12 + (r16 & 3)) : 12288 + swz(r16 - 8));
    unsigned aX2 = ldsP + (2 + (r16 >> 2)) * 2048 + swz(12 + (r16 & 3));
    unsigned aD0k = aD0 ^ 64, aD1k = aD1 ^ 64, aX1k = aX1 ^ 64, aX2k = aX2 ^ 64;
    asm volatile("" : "+v"(aF0), "+v"(aF1), "+v"(aD0), "+v"(aD1), "+v"(aX1), "+v"(aX2), "+v"(aD0k), "+v"(aD1k), "+v"(aX1k), "+v"(aX2k));
    auto ldf = [&](unsigned a) { return *(LAS const u32x4*)(size_t)a; };
    const unsigned tailmask = r8 < 4 ? 0u : 0x80000000u;

    int ip[5];
#define LDA(J) ia[0] = ldi(aLo + 4 * (J)); ia[1] = ldi(aLo + 4 * (J) + 32 * QP); ia[2] = ldi(aHi + 4 * (J)); ia[3] = ldi(aHi + 4 * (J) + 32 * OP);
#define LDP(S, G) _Pragma("unroll") for (int i = 0; i < 4; ++i) ip[i] = ldi(aP + 4 * OP * (S) + 128 * G + 32 * i); \
                  if constexpr (G == 0) ip[4] = ldi(aP + 4 * OP * (S) + 384);
#define DP(G, I) { SB dma16((((unsigned)ip[I] << 7) + ((I & 1) ? chO : chE)) | PKILL, srdT, ldsP + G * 4096 + I * 1024); SB }
#define DT { SB dma16((((unsigned)ip[4] << 7) + chE) | tailmask | PKILL, srdT, ldsP + 12288); SB }
#define DP4(G) DP(G, 0) DP(G, 1) DP(G, 2) DP(G, 3)
    static_assert(MID == 8, "index tables");
    waitv<8>();
    TL(2)
    bar();
    const u32x2 tk = *(LAS const u32x2*)(lds + TOK_OFF + p * 8);
    const unsigned tlo = __builtin_amdgcn_readfirstlane(tk[0]), thi = __builtin_amdgcn_readfirstlane(tk[1]);
    f32x4 wp[23];
    {
      LAS const char* wl = lds + WPL_OFF;
      u32x4 R[6][4];
#pragma unroll
      for (int t = 0; t < 6; ++t)
#pragma unroll
        for (int kb = 0; kb < 4; ++kb) {
          LAS const char* rp = wl + (16 * t + r16) * 200 + 16 * g4 + 64 * kb;
          const u32x2 lo = *(LAS const u32x2*)rp, hi = *(LAS const u32x2*)(rp + 8);
          R[t][kb] = u32x4{lo[0], lo[1], hi[0], hi[1]};
        }
      auto one = [&](int k) {
        u32x4 v;
#pragma unroll
        for (int i = 0; i < 4; ++i) { const int d = k - 8 * g4 - 2 * i; v[i] = d == 0 ? 0x00003F80u : d == 1 ? 0x3F800000u : 0u; }
        return v;
      };
      auto w4 = [&](int row, int col0) {
        const u32x2 v = *(LAS const u32x2*)(wl + row * 200 + col0 * 2);
        return f32x4{__builtin_bit_cast(float, v[0] << 16), __builtin_bit_cast(float, v[0] & 0xffff0000u),
                     __builtin_bit_cast(float, v[1] << 16), __builtin_bit_cast(float, v[1] & 0xffff0000u)};
      };
      const f32x4 z = f32x4{0.f, 0.f, 0.f, 0.f};
      const u32x4 sel[2] = {one(r16), one(16 + r16)};
      const u32x4 selB[2] = {one(r16 < 12 ? r16 : -9), one(r16 < 12 ? 16 + r16 : -9)}, selT = one(r16 < 12 ? -9 : r16 - 12);
#pragma unroll
      for (int q = 1; q < 6; ++q)
#pragma unroll
        for (int u = 0; u < q; ++u) wp[q * (q - 1) / 2 + u] = MFMA(sel[u & 1], R[q][u >> 1], MFMA(R[u][q >> 1], sel[q & 1], z));
#pragma unroll
      for (int t = 0; t < 6; ++t) {
        const f32x4 d1 = MFMA(R[t][3], selT, MFMA(R[t][t >> 1], selB[t & 1], z));
        wp[15 + t] = w4(r16 < 12 ? 16 * t + r16 : 84 + r16, 16 * t + 4 * g4) + (g4 == 3 || r16 >= 12 ? d1 : z);
      }
      const bool same = g4 == (r16 >> 2);
      const int f1 = g4 < 2 ? 16 * g4 + 12 : 96, f2 = 16 * g4 + 44;
      wp[21] = same && g4 < 3 ? w4(f1 + (r16 & 3), f1) : z;
      wp[22] = same ? w4(f2 + (r16 & 3), f2) : z;
#pragma unroll
      for (int t = 0; t < 23; ++t) asm volatile("" : "+v"(wp[t]));
    }
    stage_idx();
    waitv<10>();
    TL(3)
    bar();
    TL(4)
    EA(2) DA4(2)
    EA(3) DA4(3)
    waitv<4>(); LDA(4) bar();
    static_assert(G0 >= 3, "start-up schedule");
#pragma unroll
    for (int n = 1; n < G0 - 2; ++n) { DA4(n + 3) LDA(n + 4) waitv<4>(); bar(); }
    LDP(0, 0) DA4(G0 + 1) DP4(0) DT LDA(G0 + 2) LDP(0, 1) waitv<9>();
    TL(5)
    bar();
    DA4(G0 + 2) DP4(1) LDA(G0 + 3) LDP(0, 2) waitv<8>(); bar();

    u32x4 fr[9][2];
#pragma unroll
    for (int t = 2; t < 6; ++t) { fr[t][0] = u32x4{0, 0, 0, 0}; fr[t][1] = u32x4{0, 0, 0, 0}; }
    float ps0 = 0.f, ps1 = 0.f, ps2 = 0.f, ps3 = 0.f;
    f32x4 gq[2][2];
    auto pm = [&](auto cc, auto u0c, auto q0c, auto u1c, auto q1c) {
      constexpr int c = decltype(cc)::value, u0 = decltype(u0c)::value, q0 = decltype(q0c)::value, u1 = decltype(u1c)::value, q1 = decltype(q1c)::value;
      const f32x4 z = f32x4{0.f, 0.f, 0.f, 0.f};
      SB
      __builtin_amdgcn_s_setprio(PM_PRIO);
      gq[c][0] = MFMA(fr[u0][0], fr[q0][0], z);
      if constexpr (u1 >= 0) gq[c][1] = MFMA(fr[u1][0], fr[q1][0], z);
      gq[c][0] = MFMA(fr[u0][1], fr[q0][1], gq[c][0]);
      if constexpr (u1 >= 0) gq[c][1] = MFMA(fr[u1][1], fr[q1][1], gq[c][1]);
      SB __builtin_amdgcn_s_setprio(PF_PRIO);
      SB
    };
    auto pf = [&](auto cc, auto w0c, auto w1c) {
      constexpr int c = decltype(cc)::value, i0 = decltype(w0c)::value, i1 = decltype(w1c)::value;
      const f32x4 w0 = wp[i0];
      ps0 = __builtin_fmaf(gq[c][0][0], w0[0], ps0); ps1 = __builtin_fmaf(gq[c][0][1], w0[1], ps1);
      ps2 = __builtin_fmaf(gq[c][0][2], w0[2], ps2); ps3 = __builtin_fmaf(gq[c][0][3], w0[3], ps3);
      if constexpr (i1 >= 0) {
        const f32x4 w1v = wp[i1];
        ps0 = __builtin_fmaf(gq[c][1][0], w1v[0], ps0); ps1 = __builtin_fmaf(gq[c][1][1], w1v[1], ps1);
        ps2 = __builtin_fmaf(gq[c][1][2], w1v[2], ps2); ps3 = __builtin_fmaf(gq[c][1][3], w1v[3], ps3);
      }
      SB
    };
#define PM(C, U0, Q0, U1, Q1) pm(IC(C), IC(U0), IC(Q0), IC(U1), IC(Q1));
#define PF(C, I0, I1) pf(IC(C), IC(I0), IC(I1));
#define WO(U, Q) (Q * (Q - 1) / 2 + U)
    float pv;
    auto total0 = [&]() {
      asm("" : "+v"(ps0)); asm("" : "+v"(ps1)); asm("" : "+v"(ps2)); asm("" : "+v"(ps3));
      pv = (ps0 + ps1) + (ps2 + ps3);
      asm volatile("" : "+v"(pv));
      ps0 = ps1 = ps2 = ps3 = 0.f;
    };
    auto total1 = [&](int s) {
      float tot = wave_sum63(pv);
      if (lane == 63 && s >= 0) red[256 + 16 * p + s] = tot;
    };
#define RDF(D, T) fr[D][0] = ldf(aF0 + T * 2048); fr[D][1] = ldf(aF1 + T * 2048);
#define RDX(D, A) fr[D][0] = ldf(A); fr[D][1] = ldf(A##k);
#define RDD(T) if (r16 < 12) { fr[6][0] = ldf(aD0 + T * 2048); fr[6][1] = ldf(aD0k + T * 2048); \
                              fr[7][0] = ldf(aD0 + (T + 1) * 2048); fr[7][1] = ldf(aD0k + (T + 1) * 2048); }
    auto xpoll = [&]() {
      GAS u64* pf = (GAS u64*)(ws + XFLAG_OFF + (bid ^ 8) * 256 + p * 64);
      const u64 tok = ((u64)thi << 32) | tlo;
      for (unsigned spins = 0; spins < (1u << 18); ++spins) {
        if (__all(__hip_atomic_load(pf, RLX_AGENT) == tok)) break;
        __builtin_amdgcn_s_sleep(1);
      }
      asm volatile("" ::: "memory");
    };
    auto steps = [&](int s, auto fac, auto nbarc, auto lastc) {
      constexpr bool fa = decltype(fac)::value, last = decltype(lastc)::value;
      constexpr int nbar = decltype(nbarc)::value;
      const int n = 3 * s + G0;
#define XA(J, I) if constexpr (fa) DA(J, I)
#define XP(G, I) if constexpr (!last) DP(G, I)
#define ENDSTEP(CNT, K) { float x0 = ps0, x1 = ps1, x2 = ps2, x3 = ps3; asm volatile("" : "+v"(x0), "+v"(x1), "+v"(x2), "+v"(x3)); ps0 = x0; ps1 = x1; ps2 = x2; ps3 = x3; } \
      STAMP(1) waitv<CNT>(); STAMP(2) if constexpr (nbar == 2 && K == 1) xpoll(); if constexpr (nbar > K) bar(); else { SB asm volatile("s_waitcnt lgkmcnt(0)" ::: "memory"); SB } STAMP(3)
      asm volatile("" : "+v"(aLo), "+v"(aHi), "+v"(aP));
      RDF(0, 0) RDF(1, 1) RDX(6, aD0) RDX(7, aD1) RDX(8, aX1)
      PM(0, 4, 5, 3, 5) XA(n + 3, 0) DP(2, 0)
      PM(1, 3, 4, 2, 5) PF(0, WO(4, 5), WO(3, 5)) XA(n + 3, 1) DP(2, 1)
      PM(0, 0, 1, 0, 6) PF(1, WO(3, 4), WO(2, 5)) total0(); if constexpr (!TOTB) total1(s - 1); XA(n + 3, 2) DP(2, 2)
      PM(1, 1, 7, 8, 8) PF(0, WO(0, 1), 15) XA(n + 3, 3) DP(2, 3) PF(1, 16, 21)
      if constexpr (fa) { LDA(G0 + 4) }
      if constexpr (!last) { LDP(1, 0) }
      ENDSTEP((fa ? 8 : 4), 0)
      RDF(2, 2) RDF(3, 3) RDD(2)
      XA(n + 4, 0) XP(0, 0)
      if constexpr (TOTB) total1(s - 1);
      PM(0, 0, 2, 1, 2) XA(n + 4, 1) XP(0, 1)
      PM(1, 0, 3, 1, 3) PF(0, WO(0, 2), WO(1, 2)) XA(n + 4, 2) XP(0, 2)
      PM(0, 2, 3, 2, 6) PF(1, WO(0, 3), WO(1, 3)) XA(n + 4, 3) XP(0, 3)
      PM(1, 3, 7, -1, -1) PF(0, WO(2, 3), 17)
      if constexpr (!last) DT
      PF(1, 18, -1)
      if constexpr (fa) { LDA(G0 + 5) }
      if constexpr (!last) { LDP(1, 1) }
      ENDSTEP(((fa ? 4 : 0) + (last ? 0 : 5)), 1)
      RDX(8, aX2) RDF(4, 4) RDF(5, 5) RDD(4)
      XA(n + 5, 0)
      PM(0, 8, 8, 0, 4) XP(1, 0) XA(n + 5, 1)
      PM(1, 1, 4, 2, 4) PF(0, 22, WO(0, 4)) XP(1, 1) XA(n + 5, 2)
      PM(0, 0, 5, 1, 5) PF(1, WO(1, 4), WO(2, 4)) XP(1, 2) XA(n + 5, 3)
      PM(1, 4, 6, 5, 7) PF(0, WO(0, 5), WO(1, 5)) XP(1, 3) PF(1, 19, 20)
      if constexpr (fa) { LDA(G0 + 6) }
      if constexpr (!last) { LDP(1, 2) }
      aLo += 12; aHi += 12; aP += 4 * OP;
      ENDSTEP(((fa ? 4 : 0) + (last ? 0 : 4)), 2)
    };
    for (int s = 0; s < S2 - 1; ++s) steps(s, T{}, IC(3), Fa{});
    if constexpr (POST == 0) steps(15, Fa{}, IC(2), T{});
    else {
      steps(S2 - 1, Fa{}, IC(2), Fa{});
      for (int s = S2; s < 15; ++s) steps(s, Fa{}, IC(0), Fa{});
      steps(15, Fa{}, IC(0), T{});
    }
    PM(0, 4, 5, 3, 5) PM(1, 3, 4, 2, 5) PF(0, WO(4, 5), WO(3, 5)) PF(1, WO(3, 4), WO(2, 5))
    total0(); total1(15);
    if (bid == 0) {
      sweep2(rsW, WFLAG_OFF + p * 2048 + lane * 16, tlo, thi);
      sweep2(rsW, WFLAG_OFF + p * 2048 + 1024 + lane * 16, tlo, thi);
      if (lane == 0) __hip_atomic_store((GAS u64*)(ws + EPOCH_OFF + p * 256), ((u64)thi << 32) | tlo, RLX_AGENT);
    }
  } else {
    LAS float* bw = (LAS float*)(lds + BW_OFF);
    const i32x4 srdW = make_srd(ws, W1T_BYTES);
    const unsigned voffW = lane * 16;
    const unsigned wbase = h * (FB * 32768) + wid * 8192;
    WSet w0, w1s, w2s, w3;
#define W_LD4(S, OFF) w_ld<0>(S, voffW, srdW, OFF); w_ld<1>(S, voffW, srdW, OFF); w_ld<2>(S, voffW, srdW, OFF); w_ld<3>(S, voffW, srdW, OFF);
    f32x4 acc[8][4];
    u64 tok;
    {
      n_wait<4>(raw);
      asm volatile("" : "+v"(ep));
      TL(1)
      WSet o; n_frag(raw, o);
      const unsigned dst = bid < 200 ? (bid >> 1) * 32768 + wid * 8192 + (bid & 1) * 4096 + lane * 16 : 0x80000000u;
#pragma unroll
      for (int c = 0; c < 4; ++c) __builtin_amdgcn_raw_buffer_store_b128(o.r[c], rsW, dst + c * 1024, 0, 16);
      const u64 e = ((u64)(unsigned)__builtin_amdgcn_readfirstlane((int)ep[1]) << 32) | (unsigned)__builtin_amdgcn_readfirstlane((int)ep[0]);
      tok = (e ^ 0x9E3779B97F4A7C15ull) * 0xBF58476D1CE4E5B9ull + 0x632BE59BD9B4E019ull;
      if (tok == e) tok = ~e;
      asm volatile("s_waitcnt vmcnt(0)" : "+v"(bq0), "+v"(bq1), "+v"(w2v), "+v"(b2v) :: "memory");
      TL(2)
      if (lane == 0) {
        __hip_atomic_store((GAS u64*)(ws + WFLAG_OFF + wid * 2048 + bid * 8), tok, RLX_AGENT);
        *(LAS u64*)(lds + TOK_OFF + wid * 8) = tok;
      }
      bw[wid * 64 + lane] = __builtin_bit_cast(float, w2v << 16);
      if (tid == 0) bw[256] = __builtin_bit_cast(float, b2v << 16);
#pragma unroll
      for (int m = 0; m < 8; ++m)
#pragma unroll
        for (int nn = 0; nn < 4; ++nn) {
#pragma unroll
          for (int r = 0; r < 4; ++r) {
            const unsigned d = (r < 2 ? bq0 : bq1)[(r & 1) * 2 + (nn >> 1)];
            acc[m][nn][r] = m < 4 ? 0.f : __builtin_bit_cast(float, (nn & 1) ? (d & 0xffff0000u) : (d << 16));
          }
          asm volatile("" : "+v"(acc[m][nn]));
        }
      bar();
      waitv<0>();
      __builtin_amdgcn_s_sleep(PDELAY);
      TL(3)
      sweep2(rsW, WFLAG_OFF + wid * 2048 + h * (2 * MAINS * 8) + (lane < FB ? lane : FB - 1) * 16, (unsigned)tok, (unsigned)(tok >> 32));
      TL(4)
    }
    W_LD4(w0, wbase) W_LD4(w1s, wbase + 4096) W_LD4(w2s, wbase + 32768)
    TL(5)
    bar();
    TL(6)
    u32x4 a[4];
    unsigned b0 = frag0, b1a = frag1;
#pragma unroll
    for (int m = 0; m < 3; ++m) a[m] = *(LAS u32x4*)(lds + b0 + m * 2048);
#define GRP(CUR, M, RD, LD) \
      mfma1(acc[M][0], CUR.r[0], a[M & 3]); SB RD SB \
      mfma1(acc[M][1], CUR.r[1], a[M & 3]); LD \
      mfma1(acc[M][2], CUR.r[2], a[M & 3]); touch(a[(M + 1) & 3]); \
      mfma1(acc[M][3], CUR.r[3], a[M & 3]); SB
#define RDK(M) if constexpr (M < 5) a[(M + 3) & 3] = *(LAS u32x4*)(lds + cb + co + (M + 3) * 2048); \
               else a[(M + 3) & 3] = *(LAS u32x4*)(lds + nb + no + (M - 5) * 2048);
    auto kstep = [&](WSet& cur, WSet& nxt, unsigned soff, unsigned cb, auto coc, unsigned nb, auto noc) {
      constexpr int co = decltype(coc)::value, no = decltype(noc)::value;
      w_wait<8>(cur);
      SB
#define G(M) GRP(cur, M, RDK(M), if constexpr ((M & 1) == 0) w_ld<M / 2>(nxt, voffW, srdW, soff);)
      G(0) G(1) G(2) G(3) G(4) G(5) G(6) G(7)
#undef G
    };
    unsigned so = wbase + 32768 + 4096;
    const unsigned wmid = MAINS * 32768 + wid * 8192;
    for (int n = 0; n < MAINS; n += 2) {
      kstep(w0, w3, so, b0, IC(0), b1a, IC(0));
      if (n == MAINS - 2) so = wmid + t_woff(0) - 28672;
      kstep(w1s, w0, so + 28672, b1a, IC(0), b0, IC(A_STAGE));
      STAMP(0) bar_nw(); STAMP(3)
      kstep(w2s, w1s, so + 32768, b0, IC(A_STAGE), b1a, IC(A_STAGE));
      b0 ^= 2 * A_STAGE;
      if (n == MAINS - 2) so = wmid + t_woff(2) - 32768 - 28672;
      kstep(w3, w2s, so + 32768 + 28672, b1a, IC(A_STAGE), b0, IC(0));
      b1a ^= 2 * A_STAGE;
      STAMP(0) bar_nw(); STAMP(3)
      so += 65536;
    }
    u32x4 px[8];
    {
      const i32x4 srdS = make_srd(ws + SLAB_OFF + (size_t)bid * SLAB_BYTES, SLAB_BYTES);
      const i32x4 srdR = make_srd(ws + SLAB_OFF + (size_t)(bid ^ 8) * SLAB_BYTES, SLAB_BYTES);
      WSet ts[NU];
      ts[0] = w0; ts[1] = w1s; ts[2] = w2s;
      auto top = [&](auto uc, auto gc) {
        constexpr int u = decltype(uc)::value, g = decltype(gc)::value, st = t_store(u, g), ld = t_load(u, g), x = t_w0(u) + t_wn(u) * g;
        if constexpr (st >= 0) {
          const f32x4 lo = acc[st >> 1][2 * (st & 1)], hi = acc[st >> 1][2 * (st & 1) + 1];
          x_st<st & 3>(u32x4{pk_bf16(lo[0], lo[1]), pk_bf16(lo[2], lo[3]), pk_bf16(hi[0], hi[1]), pk_bf16(hi[2], hi[3])}, voffW, srdS, wid * 8192 + (st >> 2) * 4096);
        }
        if constexpr (ld >= 0) x_ld<ld & 3>(px[ld], voffW, srdR, wid * 8192 + (ld >> 2) * 4096);
        if constexpr (t_wn(u) > 0) w_ld<x & 3>(ts[x >> 2], voffW, srdW, wmid + t_woff(x >> 2));
        if constexpr (t_wn(u) > 1) w_ld<(x + 1) & 3>(ts[x >> 2], voffW, srdW, wmid + t_woff(x >> 2));
      };
      auto hk = [&](auto uc) {
        constexpr int U = decltype(uc)::value;
        constexpr int co = ((MAINS + U / 4) & 3) * A_STAGE + ((U >> 1) & 1) * 8192;
        constexpr int no = ((MAINS + (U + 1) / 4) & 3) * A_STAGE + (((U + 1) >> 1) & 1) * 8192;
        const unsigned cb = (U & 1) ? frag1 : frag0, nb = (U & 1) ? frag0 : frag1;
        WSet& cur = ts[U];
        w_wait<tail_wait(U)>(cur);
        if constexpr (U == FLAG_U) {
          if (lane == 0) __hip_atomic_store((GAS u64*)(ws + XFLAG_OFF + bid * 256 + wid * 64), tok, RLX_AGENT);
        }
        SB
#define RDR(M) if constexpr (M == 0) a[3] = *(LAS u32x4*)(lds + cb + co + 3 * 2048); \
               else if constexpr (U + 1 < NU) a[M - 1] = *(LAS u32x4*)(lds + nb + no + (M - 1) * 2048);
        GRP(cur, 4, RDR(0), top(uc, IC(0));) GRP(cur, 5, RDR(1), top(uc, IC(1));) GRP(cur, 6, RDR(2), top(uc, IC(2));) GRP(cur, 7, RDR(3), top(uc, IC(3));)
      };
#define HK4(J) hk(IC(4 * J)); hk(IC(4 * J + 1)); hk(IC(4 * J + 2)); hk(IC(4 * J + 3));
      HK4(0) bar_nw(); HK4(1) bar_nw(); HK4(2) bar_nw();
      HK4((MID / 2 - 1))
      asm volatile("s_nop 7"); SB
    }
    __builtin_amdgcn_s_setprio(2);
    f32x4 ww[4];
#pragma unroll
    for (int r = 0; r < 4; ++r) ww[r] = *(LAS f32x4*)(bw + wid * 64 + g4 * 16 + r * 4);
    f32x4 wt[4];
#pragma unroll
    for (int nn = 0; nn < 4; ++nn) wt[nn] = f32x4{ww[0][nn], ww[1][nn], ww[2][nn], ww[3][nn]};
#pragma unroll
    for (int m = 0; m < 4; ++m) {
      float s = 0.f;
      if (m == 0) x_wait<6>(px[0], px[1]); else if (m == 1) x_wait<4>(px[2], px[3]); else if (m == 2) x_wait<2>(px[4], px[5]); else x_wait<0>(px[6], px[7]);
      f32x2 s2 = f32x2{0.f, 0.f};
#pragma unroll
      for (int nn = 0; nn < 4; ++nn)
#pragma unroll
        for (int r = 0; r < 4; r += 2) {
          const unsigned pd = px[2 * m + (nn >> 1)][2 * (nn & 1) + (r >> 1)];
          const f32x2 x = f32x2{acc[4 + m][nn][r], acc[4 + m][nn][r + 1]} + f32x2{__builtin_bit_cast(float, pd << 16), __builtin_bit_cast(float, pd & 0xffff0000u)};
          const f32x2 t = __builtin_elementwise_fma(x * x, f32x2{-0.1029432397f, -0.1029432397f}, f32x2{-2.302208198f, -2.302208198f});
          const f32x2 z = x * t;
          const f32x2 d = f32x2{__builtin_amdgcn_exp2f(z[0]), __builtin_amdgcn_exp2f(z[1])} + f32x2{1.f, 1.f};
          const f32x2 y = x * f32x2{wt[nn][r], wt[nn][r + 1]};
          s2 = __builtin_elementwise_fma(y, f32x2{__builtin_amdgcn_rcpf(d[0]), __builtin_amdgcn_rcpf(d[1])}, s2);
        }
      s = s2[0] + s2[1];
      s += __shfl_xor(s, 16);
      s += __shfl_xor(s, 32);
      if (lane < 16) red[wid * 64 + m * 16 + lane] = s;
    }
  }
  bar();
  if (tid < 64) {
    float v = red[256 + tid] + ((red[tid] + red[64 + tid]) + (red[128 + tid] + red[192 + tid])) + *(LAS const float*)(lds + BW_OFF + 1024);
    __hip_atomic_store((GAS unsigned short*)out + sown + tid, (unsigned short)pk_bf16(v, 0.f), RLX_AGENT);
  }
}

extern "C" void kernel_launch(void* const* d_in, const int* in_sizes, int n_in, void* d_out, int out_size, void* d_ws, size_t ws_size,
                              hipStream_t stream) {
  (void)hipFuncSetAttribute((const void*)pnn_kernel, hipFuncAttributeMaxDynamicSharedMemorySize, LDS_BYTES);
  pnn_kernel<<<NWG, 512, LDS_BYTES, stream>>>((const int*)d_in[0], (const char*)d_in[1], (u64)in_sizes[1] * 2ull, (const char*)d_in[2],
                                              (const char*)d_in[3], (const unsigned short*)d_in[4], (const unsigned short*)d_in[5],
                                              (const unsigned short*)d_in[6], (char*)d_ws, (__hip_bfloat16*)d_out);
}
```

```cpp
#include <hip/hip_runtime.h>
#include <hip/hip_bf16.h>
#include <stdint.h>
#include <type_traits>

typedef float f32x4 __attribute__((ext_vector_type(4)));
typedef float f32x2 __attribute__((ext_vector_type(2)));
typedef int i32x4 __attribute__((ext_vector_type(4)));
typedef unsigned u32x4 __attribute__((ext_vector_type(4)));
typedef unsigned u32x2 __attribute__((ext_vector_type(2)));
typedef __bf16 bf16x8 __attribute__((ext_vector_type(8)));
typedef unsigned long long u64;
#define LAS __attribute__((address_space(3)))
#define GAS __attribute__((address_space(1)))

constexpr int NB = 16384, NF = 100, ND = 64, NH = 256, HF = 50;
constexpr int NWG = 256;
constexpr int MID = 8, MAINS = (NF - MID) / 2, FB = MAINS + MID, NU = 2 * MID;
constexpr int G0 = 4;
constexpr int FLAG_U = 6;
constexpr int t_store(int u, int g) { return u < 3 ? ((g & 1) ? 2 * u + (g >> 1) : -1) : (u < 5 && g == 1 ? u + 3 : -1); }
constexpr int t_load(int u, int g) { return u >= NU - 4 && (g & 1) ? 2 * (u - NU + 4) + (g >> 1) : -1; }
constexpr int t_iss(int v) {
  if (v < 3) return -1;
  if (v <= FLAG_U + 3) { const int x = FLAG_U - 2 - (FLAG_U + 3 - v) / 2; return v - 3 < x ? v - 3 : x; }
  { const int x = FLAG_U + (v - FLAG_U - 4) / 2; return x > v - 7 ? x : v - 7; }
}
constexpr int t_woff(int v) { return ((v >> 2) + ((v & 2) ? 0 : 4)) * 32768 + (v & 1) * 4096; }
constexpr int t_wn(int u) { int c = 0; for (int v = 3; v < NU; ++v) c += t_iss(v) == u; return c; }
constexpr int t_w0(int u) { for (int v = 3; v < NU; ++v) if (t_iss(v) == u) return 4 * v; return 0; }
constexpr int tail_wait(int u) {
  if (u == FLAG_U) return 0;
  int cnt = u < 3 ? 4 * (2 - u) : 0;
  for (int v = 0; v < u; ++v) {
    if (v == FLAG_U) ++cnt;
    for (int g = 0; g < 4; ++g) {
      cnt += (t_store(v, g) >= 0) + (t_load(v, g) >= 0);
      if (t_wn(v) > 0 && t_w0(v) + t_wn(v) * (g + 1) - 1 == 4 * u + 3) cnt = 0; else cnt += t_wn(v);
    }
  }
  return cnt;
}
constexpr int W1T_BYTES = NF * ND * NH * 2;
constexpr int EPOCH_OFF = W1T_BYTES;
constexpr int WFLAG_OFF = EPOCH_OFF + 1024;
constexpr int XFLAG_OFF = WFLAG_OFF + 8192;
constexpr int SLAB_OFF = XFLAG_OFF + NWG * 256;
constexpr int SLAB_BYTES = 32768;
constexpr int A_STAGE = 16384;
constexpr int P_OFF = 4 * A_STAGE;
constexpr int P_WAVE = 13 * 1024;
constexpr int WPL_OFF = 2 * A_STAGE;
constexpr int OP = 104;
constexpr int IDXO_OFF = P_OFF + 4 * P_WAVE;
constexpr int IDXP_OFF = IDXO_OFF + 64 * OP * 4;
constexpr int QP = 48;
constexpr int RED_OFF = IDXP_OFF + 64 * QP * 4;
constexpr int BW_OFF = RED_OFF + 64 * 4;
constexpr int TOK_OFF = BW_OFF + 260 * 4;
constexpr int LDS_BYTES = TOK_OFF + 32;
static_assert(LDS_BYTES <= 160 * 1024, "LDS");

__device__ __forceinline__ void dma16(unsigned voff, i32x4 srd, unsigned lds_dst) {
  asm volatile("s_mov_b32 m0, %0\n\ts_nop 0\n\tbuffer_load_dwordx4 %1, %2, 0 offen lds" :: "s"(lds_dst), "v"(voff), "s"(srd) : "memory");
}
template <int OFF> __device__ __forceinline__ void gat16(int row, unsigned ch, i32x4 srd, unsigned base) {
  unsigned t;
  asm volatile("s_add_u32 m0, %1, %5\n\tv_lshl_or_b32 %0, %2, 7, %3\n\tbuffer_load_dwordx4 %0, %4, 0 offen lds"
               : "=&v"(t) : "s"(base), "v"(row), "v"(ch), "s"(srd), "i"(OFF) : "memory", "scc");
}
template <int N> __device__ __forceinline__ void waitv() { asm volatile("s_waitcnt vmcnt(%0)" :: "i"(N) : "memory"); }
#define SB __builtin_amdgcn_sched_barrier(0);
__device__ __forceinline__ void bar() {
  SB asm volatile("s_waitcnt lgkmcnt(0)" ::: "memory");
  __builtin_amdgcn_s_barrier();
  asm volatile("" ::: "memory"); SB
}
__device__ __forceinline__ void bar_nw() {
  SB __builtin_amdgcn_s_barrier();
  asm volatile("" ::: "memory"); SB
}
__device__ __forceinline__ i32x4 make_srd(const void* p, unsigned bytes) {
  u64 a = (u64)p;
  i32x4 r;
  r[0] = __builtin_amdgcn_readfirstlane((int)(unsigned)a);
  r[1] = __builtin_amdgcn_readfirstlane((int)(unsigned)(a >> 32) & 0xffff);
  r[2] = (int)bytes;
  r[3] = 0x00020000;
  return r;
}
#define MFMA(a, b, c) __builtin_amdgcn_mfma_f32_16x16x32_bf16(__builtin_bit_cast(bf16x8, a), __builtin_bit_cast(bf16x8, b), c, 0, 0, 0)
#define IC(x) std::integral_constant<int, x>{}
#define RLX_AGENT __ATOMIC_RELAXED, __HIP_MEMORY_SCOPE_AGENT

struct WSet { u32x4 r[4]; };
template <int J> __device__ __forceinline__ void w_ld(WSet& s, unsigned voff, i32x4 srd, unsigned soff) {
  asm volatile("buffer_load_dwordx4 %0, %1, %2, %3 offen offset:%4 sc1" : "=v"(s.r[J]) : "v"(voff), "s"(srd), "s"(soff), "i"(J * 1024) : "memory");
}
template <int N> __device__ __forceinline__ void w_wait(WSet& s) {
  asm volatile("s_waitcnt vmcnt(%4)" : "+v"(s.r[0]), "+v"(s.r[1]), "+v"(s.r[2]), "+v"(s.r[3]) : "i"(N) : "memory");
}
template <int J> __device__ __forceinline__ void x_st(const u32x4& v, unsigned voff, i32x4 srd, unsigned soff) {
  asm volatile("buffer_store_dwordx4 %0, %1, %2, %3 offen offset:%4 sc1\n\ts_nop 1" :: "v"(v), "v"(voff), "s"(srd), "s"(soff), "i"(J * 1024) : "memory");
}
template <int J> __device__ __forceinline__ void x_ld(u32x4& v, unsigned voff, i32x4 srd, unsigned soff) {
  asm volatile("buffer_load_dwordx4 %0, %1, %2, %3 offen offset:%4 sc1" : "=v"(v) : "v"(voff), "s"(srd), "s"(soff), "i"(J * 1024) : "memory");
}
template <int N> __device__ __forceinline__ void x_wait(u32x4& c0, u32x4& c1) {
  asm volatile("s_waitcnt vmcnt(%2)" : "+v"(c0), "+v"(c1) : "i"(N) : "memory");
}
__device__ __forceinline__ unsigned pk_bf16(float lo, float hi) {
  typedef __bf16 bf16x2 __attribute__((ext_vector_type(2)));
  return __builtin_bit_cast(unsigned, bf16x2{(__bf16)lo, (__bf16)hi});
}
__device__ __forceinline__ void mfma1(f32x4& c, const u32x4& w, const u32x4& e) {
  asm volatile("v_mfma_f32_16x16x32_bf16 %0, %1, %2, %0" : "+v"(c) : "v"(w), "v"(e));
}
__device__ __forceinline__ void touch(const u32x4& x) { asm volatile("" :: "v"(x)); }
struct NSet { u32x2 r[8]; };
template <int J> __device__ __forceinline__ void n_ld(NSet& s, unsigned voff, i32x4 srd, unsigned soff) {
  asm volatile("buffer_load_dwordx2 %0, %1, %2, %3 offen offset:%4" : "=v"(s.r[J]) : "v"(voff), "s"(srd), "s"(soff), "i"(J * 512) : "memory");
}
template <int N> __device__ __forceinline__ void n_wait(NSet& s) {
  asm volatile("s_waitcnt vmcnt(%8)"
               : "+v"(s.r[0]), "+v"(s.r[1]), "+v"(s.r[2]), "+v"(s.r[3]), "+v"(s.r[4]), "+v"(s.r[5]), "+v"(s.r[6]), "+v"(s.r[7]) : "i"(N) : "memory");
}
__device__ __forceinline__ void n_frag(const NSet& s, WSet& o) {
#pragma unroll
  for (int c = 0; c < 4; ++c)
#pragma unroll
    for (int i = 0; i < 4; ++i) o.r[c][i] = __builtin_amdgcn_perm(s.r[2 * i + 1][c >> 1], s.r[2 * i][c >> 1], (c & 1) ? 0x07060302u : 0x05040100u);
}
#define N_LD8(S, V, OFF) n_ld<0>(S, V, srdN, OFF); n_ld<1>(S, V, srdN, OFF); n_ld<2>(S, V, srdN, OFF); n_ld<3>(S, V, srdN, OFF); \
                         n_ld<4>(S, V, srdN, OFF); n_ld<5>(S, V, srdN, OFF); n_ld<6>(S, V, srdN, OFF); n_ld<7>(S, V, srdN, OFF);
template <int CTRL, int RM> __device__ __forceinline__ float dpp_add(float v) {
  return v + __builtin_bit_cast(float, __builtin_amdgcn_update_dpp(0, __builtin_bit_cast(int, v), CTRL, RM, 0xf, true));
}
__device__ __forceinline__ float wave_sum63(float v) {
  v = dpp_add<0x111, 0xf>(v); v = dpp_add<0x112, 0xf>(v); v = dpp_add<0x114, 0xf>(v); v = dpp_add<0x118, 0xf>(v);
  v = dpp_add<0x142, 0xa>(v); v = dpp_add<0x143, 0xc>(v);
  return v;
}
__device__ __forceinline__ void sweep2(__amdgpu_buffer_rsrc_t rs, unsigned off, unsigned tlo, unsigned thi) {
  for (unsigned spins = 0; spins < (1u << 18); ++spins) {
    asm volatile("" ::: "memory");
    const u32x4 v = __builtin_amdgcn_raw_buffer_load_b128(rs, off, 0, 16);
    if (__all(v[0] == tlo && v[1] == thi && v[2] == tlo && v[3] == thi)) break;
    __builtin_amdgcn_s_sleep(8);
  }
  asm volatile("" ::: "memory");
}

__global__ __launch_bounds__(512, 2) void pnn_kernel(const int* __restrict__ idx, const char* __restrict__ table, u64 table_bytes,
                                                     const char* __restrict__ wpg, const char* __restrict__ w1,
                                                     const unsigned short* __restrict__ b1, const unsigned short* __restrict__ w2,
                                                     const unsigned short* __restrict__ b2, char* __restrict__ ws,
                                                     __hip_bfloat16* __restrict__ out) {
  extern __shared__ __attribute__((aligned(16))) char lds_raw[];
  LAS char* lds = (LAS char*)lds_raw;
  const int tid = threadIdx.x, lane = tid & 63, wid = __builtin_amdgcn_readfirstlane(tid >> 6);
  const int bid = blockIdx.x, h = (bid >> 3) & 1;
  const int pair = (bid & 7) * 16 + (bid >> 4);
  const int sown = pair * 128 + 64 * h, spar = pair * 128 + 64 * (h ^ 1);
  asm volatile("" :: "s"(idx), "s"(table), "s"(table_bytes), "s"(wpg), "s"(w1), "s"(b1), "s"(w2), "s"(b2), "s"(ws), "s"(out));
  const unsigned lds0 = (unsigned)(size_t)lds;
  const int r16 = lane & 15, g4 = lane >> 4;
  const unsigned frag0 = r16 * 128 + ((g4 ^ ((r16 >> 1) & 7)) << 4);
  const unsigned frag1 = frag0 ^ 64;
  LAS float* red = (LAS float*)(lds + RED_OFF);
  using T = std::true_type; using Fa = std::false_type;
  const __amdgpu_buffer_rsrc_t rsW = __builtin_amdgcn_make_buffer_rsrc(ws, 0, SLAB_OFF, 0x00020000);
  const i32x4 srdN = make_srd(w1, W1T_BYTES);

  const int p = wid - 4, r8 = lane >> 3;
  const i32x4 srdI = make_srd(idx, NB * NF * 4);
  const i32x4 srdT = make_srd(table, (unsigned)table_bytes);
  const unsigned chE = (unsigned)(((lane & 7) ^ (lane >> 4)) << 4);
  const unsigned chO = chE ^ 64;
  const unsigned ldsA = lds0 + p * 2048;
  int ia[4];
#define DA(J, I) { SB gat16<(I >> 1) * 8192 + (I & 1) * 1024>(ia[I], (I & 1) ? chO : chE, srdT, ldsA + ((J) & 3) * A_STAGE); SB }
#define DA4(J) DA(J, 0) DA(J, 1) DA(J, 2) DA(J, 3)
#define EA(J) ia[0] = e0[J]; ia[1] = e1[J]; ia[2] = e2[J]; ia[3] = e3[J];
  NSet raw; u32x2 ep; u32x4 bq0, bq1; unsigned w2v, b2v;
  i32x4 e0, e1, e2, e3;
  if (wid < 4) {
    N_LD8(raw, g4 * 4096 + wid * 128 + r16 * 8, (unsigned)(bid < 200 ? bid : 0) * 16384u)
    asm volatile("global_load_dwordx2 %0, %1, off sc1" : "=v"(ep) : "v"(ws + EPOCH_OFF + wid * 256) : "memory");
    asm volatile("global_load_dwordx4 %0, %1, off" : "=v"(bq0) : "v"(b1 + wid * 64 + g4 * 16) : "memory");
    asm volatile("global_load_dwordx4 %0, %1, off offset:16" : "=v"(bq1) : "v"(b1 + wid * 64 + g4 * 16) : "memory");
    asm volatile("global_load_ushort %0, %1, off" : "=v"(w2v) : "v"(w2 + wid * 64 + lane) : "memory");
    asm volatile("global_load_ushort %0, %1, off" : "=v"(b2v) : "v"(b2) : "memory");
  } else {
    const unsigned eq = (unsigned)(spar + 16 * p + r8) * 400u + FB * 4u * h, eo = (unsigned)(sown + 16 * p + r8) * 400u + FB * 4u * h;
    asm volatile("buffer_load_dwordx4 %0, %1, %2, 0 offen" : "=v"(e0) : "v"(eq), "s"(srdI) : "memory");
    asm volatile("buffer_load_dwordx4 %0, %1, %2, 0 offen offset:3200" : "=v"(e1) : "v"(eq), "s"(srdI) : "memory");
    asm volatile("buffer_load_dwordx4 %0, %1, %2, 0 offen" : "=v"(e2) : "v"(eo), "s"(srdI) : "memory");
    asm volatile("buffer_load_dwordx4 %0, %1, %2, 0 offen offset:3200" : "=v"(e3) : "v"(eo), "s"(srdI) : "memory");
    const i32x4 srdP = make_srd(wpg, NF * NF * 2);
#pragma unroll
    for (int i = 0; i < 5; ++i) dma16(p * 5120 + i * 1024 + lane * 16, srdP, lds0 + WPL_OFF + p * 5120 + i * 1024);
  }
  int role = wid;
  asm volatile("" : "+s"(role));
  if (role >= 4) {
    __builtin_amdgcn_s_setprio(1);
    asm volatile("s_waitcnt vmcnt(5)" : "+v"(e0), "+v"(e1), "+v"(e2), "+v"(e3) :: "memory");
    EA(0) DA4(0) EA(1) DA4(1)
    unsigned aLo = lds0 + IDXP_OFF + ((16 * p + r8) * QP - 2) * 4;
    unsigned aHi = lds0 + IDXO_OFF + ((16 * p + r8) * OP + FB * h) * 4;
    unsigned aP = lds0 + IDXO_OFF + (16 * p * OP + r8) * 4;
    auto ldi = [&](unsigned a) { return *(LAS const int*)(size_t)a; };
    const unsigned ldsP = lds0 + P_OFF + p * P_WAVE;
    auto swz = [&](int row) { return (unsigned)(row * 128 + ((g4 ^ ((row >> 1) & 7)) << 4)); };
    unsigned aF0 = ldsP + frag0, aF1 = ldsP + frag1;
    unsigned aD0 = ldsP + (r16 < 12 ? frag0 : 12288 + swz(r16 - 12)), aD1 = aD0 + (r16 < 12 ? 2048 : 0);
    unsigned aX1 = ldsP + (r16 < 8 ? (r16 >> 2) * 2048 + swz(12 + (r16 & 3)) : 12288 + swz(r16 - 8));
    unsigned aX2 = ldsP + (2 + (r16 >> 2)) * 2048 + swz(12 + (r16 & 3));
    unsigned aD0k = aD0 ^ 64, aD1k = aD1 ^ 64, aX1k = aX1 ^ 64, aX2k = aX2 ^ 64;
    asm volatile("" : "+v"(aF0), "+v"(aF1), "+v"(aD0), "+v"(aD1), "+v"(aX1), "+v"(aX2), "+v"(aD0k), "+v"(aD1k), "+v"(aX1k), "+v"(aX2k));
    auto ldf = [&](unsigned a) { return *(LAS const u32x4*)(size_t)a; };
    const unsigned chT = chE | (r8 < 4 ? 0u : 0x80000000u);

    int ip[5];
#define LDA(J) ia[0] = ldi(aLo + 4 * (J)); ia[1] = ldi(aLo + 4 * (J) + 32 * QP); ia[2] = ldi(aHi + 4 * (J)); ia[3] = ldi(aHi + 4 * (J) + 32 * OP);
#define LDP(S, G) _Pragma("unroll") for (int i = 0; i < 4; ++i) ip[i] = ldi(aP + 4 * OP * (S) + 128 * G + 32 * i); \
                  if constexpr (G == 0) ip[4] = ldi(aP + 4 * OP * (S) + 384);
#define DP(G, I) { SB gat16<G * 4096 + I * 1024>(ip[I], (I & 1) ? chO : chE, srdT, ldsP); SB }
#define DT { SB gat16<12288>(ip[4], chT, srdT, ldsP); SB }
#define DP4(G) DP(G, 0) DP(G, 1) DP(G, 2) DP(G, 3)
    waitv<8>();
    bar();
    const u32x2 tk = *(LAS const u32x2*)(lds + TOK_OFF + p * 8);
    const unsigned tlo = __builtin_amdgcn_readfirstlane(tk[0]), thi = __builtin_amdgcn_readfirstlane(tk[1]);
    f32x4 wp[23];
    {
      LAS const char* wl = lds + WPL_OFF;
      u32x4 R[6][4];
#pragma unroll
      for (int t = 0; t < 6; ++t)
#pragma unroll
        for (int kb = 0; kb < 4; ++kb) {
          LAS const char* rp = wl + (16 * t + r16) * 200 + 16 * g4 + 64 * kb;
          const u32x2 lo = *(LAS const u32x2*)rp, hi = *(LAS const u32x2*)(rp + 8);
          R[t][kb] = u32x4{lo[0], lo[1], hi[0], hi[1]};
        }
      auto one = [&](int k) {
        u32x4 v;
#pragma unroll
        for (int i = 0; i < 4; ++i) { const int d = k - 8 * g4 - 2 * i; v[i] = d == 0 ? 0x00003F80u : d == 1 ? 0x3F800000u : 0u; }
        return v;
      };
      auto w4 = [&](int row, int col0) {
        const u32x2 v = *(LAS const u32x2*)(wl + row * 200 + col0 * 2);
        return f32x4{__builtin_bit_cast(float, v[0] << 16), __builtin_bit_cast(float, v[0] & 0xffff0000u),
                     __builtin_bit_cast(float, v[1] << 16), __builtin_bit_cast(float, v[1] & 0xffff0000u)};
      };
      const f32x4 z = f32x4{0.f, 0.f, 0.f, 0.f};
      const u32x4 sel[2] = {one(r16), one(16 + r16)};
      const u32x4 selB[2] = {one(r16 < 12 ? r16 : -9), one(r16 < 12 ? 16 + r16 : -9)}, selT = one(r16 < 12 ? -9 : r16 - 12);
#pragma unroll
      for (int q = 1; q < 6; ++q)
#pragma unroll
        for (int u = 0; u < q; ++u) wp[q * (q - 1) / 2 + u] = MFMA(sel[u & 1], R[q][u >> 1], MFMA(R[u][q >> 1], sel[q & 1], z));
#pragma unroll
      for (int t = 0; t < 6; ++t) {
        const f32x4 d1 = MFMA(R[t][3], selT, MFMA(R[t][t >> 1], selB[t & 1], z));
        wp[15 + t] = w4(r16 < 12 ? 16 * t + r16 : 84 + r16, 16 * t + 4 * g4) + (g4 == 3 || r16 >= 12 ? d1 : z);
      }
      const bool same = g4 == (r16 >> 2);
      const int f1 = g4 < 2 ? 16 * g4 + 12 : 96, f2 = 16 * g4 + 44;
      wp[21] = same && g4 < 3 ? w4(f1 + (r16 & 3), f1) : z;
      wp[22] = same ? w4(f2 + (r16 & 3), f2) : z;
#pragma unroll
      for (int t = 0; t < 23; ++t) asm volatile("" : "+v"(wp[t]));
    }
    {
      const unsigned oo = (unsigned)(sown + 16 * p) * 400u, op = (unsigned)(spar + 16 * p) * 400u + (FB * h + 2) * 4u;
#pragma unroll
      for (int i = 0; i < 3; ++i) {
        const unsigned c = lane + 64 * i, row = c / 12, k = c % 12;
        dma16(row * 400 + (k < 11 ? op + k * 16 : oo + (MAINS + 4) * 4), srdI, lds0 + IDXP_OFF + p * (16 * QP * 4) + i * 1024);
      }
#pragma unroll
      for (int i = 0; i < 7; ++i) {
        const unsigned c = lane + 64 * i, row = c / 26, k = c % 26;
        if (i < 6 || lane < 32) dma16(oo + row * 400 + (k < 25 ? k * 16 : MAINS * 4), srdI, lds0 + IDXO_OFF + p * (16 * OP * 4) + i * 1024);
      }
    }
    waitv<10>();
    bar();
    static_assert(G0 == 4, "start-up schedule");
    __builtin_amdgcn_s_setprio(0);
    EA(2) DA4(2) EA(3) DA4(3) waitv<4>(); LDA(4) bar();
    if (bid == 0) {
      const unsigned fo = WFLAG_OFF + p * 2048 + lane * 16;
      const i32x4 srdF = make_srd(ws, SLAB_OFF);
      u32x4 fa, fb;
      x_ld<0>(fa, fo, srdF, 0); x_ld<1>(fb, fo, srdF, 0);
      DA4(4)
      x_wait<4>(fa, fb);
      if (!__all(fa[0] == tlo && fa[1] == thi && fa[2] == tlo && fa[3] == thi)) sweep2(rsW, fo, tlo, thi);
      if (!__all(fb[0] == tlo && fb[1] == thi && fb[2] == tlo && fb[3] == thi)) sweep2(rsW, fo + 1024, tlo, thi);
      if (lane == 0) __hip_atomic_store((GAS u64*)(ws + EPOCH_OFF + p * 256), ((u64)thi << 32) | tlo, RLX_AGENT);
    } else {
      DA4(4)
      waitv<4>();
    }
    LDA(5) bar();
    LDP(0, 0) DA4(5) DP4(0) DT LDA(6) LDP(0, 1) waitv<9>(); bar();
    DA4(6) DP4(1) LDA(7) LDP(0, 2) waitv<8>(); bar();

    u32x4 fr[9][2];
#pragma unroll
    for (int t = 2; t < 6; ++t) { fr[t][0] = u32x4{0, 0, 0, 0}; fr[t][1] = u32x4{0, 0, 0, 0}; }
    float ps0 = 0.f, ps1 = 0.f, ps2 = 0.f, ps3 = 0.f;
    f32x4 gq[2][2];
    auto pm = [&](auto cc, auto u0c, auto q0c, auto u1c, auto q1c) {
      constexpr int c = decltype(cc)::value, u0 = decltype(u0c)::value, q0 = decltype(q0c)::value, u1 = decltype(u1c)::value, q1 = decltype(q1c)::value;
      const f32x4 z = f32x4{0.f, 0.f, 0.f, 0.f};
      SB __builtin_amdgcn_s_setprio(1);
      gq[c][0] = MFMA(fr[u0][0], fr[q0][0], z);
      if constexpr (u1 >= 0) gq[c][1] = MFMA(fr[u1][0], fr[q1][0], z);
      gq[c][0] = MFMA(fr[u0][1], fr[q0][1], gq[c][0]);
      if constexpr (u1 >= 0) gq[c][1] = MFMA(fr[u1][1], fr[q1][1], gq[c][1]);
      SB __builtin_amdgcn_s_setprio(0); SB
    };
    auto pf = [&](auto cc, auto w0c, auto w1c) {
      constexpr int c = decltype(cc)::value, i0 = decltype(w0c)::value, i1 = decltype(w1c)::value;
      const f32x4 w0 = wp[i0];
      ps0 = __builtin_fmaf(gq[c][0][0], w0[0], ps0); ps1 = __builtin_fmaf(gq[c][0][1], w0[1], ps1);
      ps2 = __builtin_fmaf(gq[c][0][2], w0[2], ps2); ps3 = __builtin_fmaf(gq[c][0][3], w0[3], ps3);
      if constexpr (i1 >= 0) {
        const f32x4 w1v = wp[i1];
        ps0 = __builtin_fmaf(gq[c][1][0], w1v[0], ps0); ps1 = __builtin_fmaf(gq[c][1][1], w1v[1], ps1);
        ps2 = __builtin_fmaf(gq[c][1][2], w1v[2], ps2); ps3 = __builtin_fmaf(gq[c][1][3], w1v[3], ps3);
      }
      SB
    };
#define PM(C, U0, Q0, U1, Q1) pm(IC(C), IC(U0), IC(Q0), IC(U1), IC(Q1));
#define PF(C, I0, I1) pf(IC(C), IC(I0), IC(I1));
#define WO(U, Q) (Q * (Q - 1) / 2 + U)
    float pv;
    auto total0 = [&]() {
      asm("" : "+v"(ps0)); asm("" : "+v"(ps1)); asm("" : "+v"(ps2)); asm("" : "+v"(ps3));
      pv = (ps0 + ps1) + (ps2 + ps3);
      asm volatile("" : "+v"(pv));
      ps0 = ps1 = ps2 = ps3 = 0.f;
    };
    auto total1 = [&](int s) {
      float tot = wave_sum63(pv);
      if (lane == 63 && s >= 0) red[16 * p + s] = tot;
    };
#define RDF(D, T) fr[D][0] = ldf(aF0 + T * 2048); fr[D][1] = ldf(aF1 + T * 2048);
#define RDX(D, A) fr[D][0] = ldf(A); fr[D][1] = ldf(A##k);
#define RDD(T) if (r16 < 12) { fr[6][0] = ldf(aD0 + T * 2048); fr[6][1] = ldf(aD0k + T * 2048); \
                              fr[7][0] = ldf(aD0 + (T + 1) * 2048); fr[7][1] = ldf(aD0k + (T + 1) * 2048); }
    GAS u64* const xpf = (GAS u64*)(ws + XFLAG_OFF + (bid ^ 8) * 256 + p * 64);
    u32x2 xv;
    auto xpoll = [&]() {
      const u64 tok = ((u64)thi << 32) | tlo;
      asm volatile("" : "+v"(xv));
      if (!__all(xv[0] == tlo && xv[1] == thi))
        for (unsigned spins = 0; spins < (1u << 18); ++spins) {
          if (__all(__hip_atomic_load(xpf, RLX_AGENT) == tok)) break;
          __builtin_amdgcn_s_sleep(1);
        }
      asm volatile("" ::: "memory");
    };
    auto steps = [&](int s, auto n0c, auto lastc) {
      constexpr int n0 = decltype(n0c)::value;
      constexpr bool last = decltype(lastc)::value;
      const int n = 3 * s + G0;
#define FA(K) (n0 + K + 3 < HF)
#define XA(K, I) if constexpr (FA(K)) DA(n + K + 3, I)
#define XP(G, I) if constexpr (!last) DP(G, I)
#define XL(K) if constexpr (n0 + K == HF - 2) asm volatile("global_load_dwordx2 %0, %1, off sc1" : "=v"(xv) : "v"(xpf) : "memory");
#define ENDSTEP(CNT, K) { float x0 = ps0, x1 = ps1, x2 = ps2, x3 = ps3; asm volatile("" : "+v"(x0), "+v"(x1), "+v"(x2), "+v"(x3)); ps0 = x0; ps1 = x1; ps2 = x2; ps3 = x3; } \
      waitv<(FA(K) ? 4 : 0) + (CNT)>(); if constexpr (n0 + K == HF - 2) xpoll(); \
      if constexpr (n0 + K < HF - 1) bar_nw(); else { SB }
      asm volatile("" : "+v"(aLo), "+v"(aHi), "+v"(aP));
      XL(0) RDF(0, 0) RDF(1, 1) RDX(6, aD0) RDX(7, aD1) RDX(8, aX1)
      PM(0, 4, 5, 3, 5) XA(0, 0) DP(2, 0)
      PM(1, 3, 4, 2, 5) PF(0, WO(4, 5), WO(3, 5)) XA(0, 1) DP(2, 1)
      PM(0, 0, 1, 0, 6) PF(1, WO(3, 4), WO(2, 5)) total0(); XA(0, 2) DP(2, 2)
      PM(1, 1, 7, 8, 8) PF(0, WO(0, 1), 15) XA(0, 3) DP(2, 3) PF(1, 16, 21)
      if constexpr (FA(1)) { LDA(G0 + 4) }
      if constexpr (!last) { LDP(1, 0) }
      ENDSTEP(4, 0)
      XL(1) RDF(2, 2) RDF(3, 3) RDD(2)
      XA(1, 0) XP(0, 0)
      total1(s - 1);
      PM(0, 0, 2, 1, 2) XA(1, 1) XP(0, 1)
      PM(1, 0, 3, 1, 3) PF(0, WO(0, 2), WO(1, 2)) XA(1, 2) XP(0, 2)
      PM(0, 2, 3, 2, 6) PF(1, WO(0, 3), WO(1, 3)) XA(1, 3) XP(0, 3)
      PM(1, 3, 7, -1, -1) PF(0, WO(2, 3), 17)
      if constexpr (!last) DT
      PF(1, 18, -1)
      if constexpr (FA(2)) { LDA(G0 + 5) }
      if constexpr (!last) { LDP(1, 1) }
      ENDSTEP(last ? 0 : 5, 1)
      XL(2) RDX(8, aX2) RDF(4, 4) RDF(5, 5) RDD(4)
      XA(2, 0)
      PM(0, 8, 8, 0, 4) XP(1, 0) XA(2, 1)
      PM(1, 1, 4, 2, 4) PF(0, 22, WO(0, 4)) XP(1, 1) XA(2, 2)
      PM(0, 0, 5, 1, 5) PF(1, WO(1, 4), WO(2, 4)) XP(1, 2) XA(2, 3)
      PM(1, 4, 6, 5, 7) PF(0, WO(0, 5), WO(1, 5)) XP(1, 3) PF(1, 19, 20)
      if constexpr (FA(3)) { LDA(G0 + 6) }
      if constexpr (!last) { LDP(1, 2) }
      aLo += 12; aHi += 12; aP += 4 * OP;
      ENDSTEP(last ? 0 : 4, 2)
    };
    for (int s = 0; s < 14; ++s) steps(s, IC(0), Fa{});
    steps(14, IC(G0 + 42), Fa{});
    steps(15, IC(G0 + 45), T{});
    PM(0, 4, 5, 3, 5) PM(1, 3, 4, 2, 5) PF(0, WO(4, 5), WO(3, 5)) PF(1, WO(3, 4), WO(2, 5))
    total0(); total1(15);
  } else {
    LAS float* bw = (LAS float*)(lds + BW_OFF);
    const i32x4 srdW = make_srd(ws, W1T_BYTES);
    const unsigned voffW = lane * 16;
    const unsigned wbase = h * (FB * 32768) + wid * 8192;
    WSet w0, w1s, w2s, w3;
#define W_LD4(S, OFF) w_ld<0>(S, voffW, srdW, OFF); w_ld<1>(S, voffW, srdW, OFF); w_ld<2>(S, voffW, srdW, OFF); w_ld<3>(S, voffW, srdW, OFF);
    f32x4 acc[8][4];
    u64 tok;
    {
      n_wait<4>(raw);
      asm volatile("" : "+v"(ep));
      WSet o; n_frag(raw, o);
      const unsigned dst = bid < 200 ? (bid >> 1) * 32768 + wid * 8192 + (bid & 1) * 4096 + lane * 16 : 0x80000000u;
#pragma unroll
      for (int c = 0; c < 4; ++c) __builtin_amdgcn_raw_buffer_store_b128(o.r[c], rsW, dst + c * 1024, 0, 16);
      const u64 e = ((u64)(unsigned)__builtin_amdgcn_readfirstlane((int)ep[1]) << 32) | (unsigned)__builtin_amdgcn_readfirstlane((int)ep[0]);
      tok = (e ^ 0x9E3779B97F4A7C15ull) * 0xBF58476D1CE4E5B9ull + 0x632BE59BD9B4E019ull;
      if (tok == e) tok = ~e;
      asm volatile("s_waitcnt vmcnt(0)" : "+v"(bq0), "+v"(bq1), "+v"(w2v), "+v"(b2v) :: "memory");
      if (lane == 0) {
        __hip_atomic_store((GAS u64*)(ws + WFLAG_OFF + wid * 2048 + bid * 8), tok, RLX_AGENT);
        *(LAS u64*)(lds + TOK_OFF + wid * 8) = tok;
      }
      bw[wid * 64 + lane] = __builtin_bit_cast(float, w2v << 16);
      if (tid == 0) bw[256] = __builtin_bit_cast(float, b2v << 16);
#pragma unroll
      for (int m = 0; m < 8; ++m)
#pragma unroll
        for (int nn = 0; nn < 4; ++nn) {
#pragma unroll
          for (int r = 0; r < 4; ++r) {
            const unsigned d = (r < 2 ? bq0 : bq1)[(r & 1) * 2 + (nn >> 1)];
            acc[m][nn][r] = m < 4 ? 0.f : __builtin_bit_cast(float, (nn & 1) ? (d & 0xffff0000u) : (d << 16));
          }
          asm volatile("" : "+v"(acc[m][nn]));
        }
      bar();
      waitv<0>();
      __builtin_amdgcn_s_sleep(12);
      sweep2(rsW, WFLAG_OFF + wid * 2048 + h * (2 * MAINS * 8) + (lane < FB ? lane : FB - 1) * 16, (unsigned)tok, (unsigned)(tok >> 32));
    }
    W_LD4(w0, wbase) W_LD4(w1s, wbase + 4096) W_LD4(w2s, wbase + 32768)
    bar();
    u32x4 a[4];
    unsigned b0 = lds0 + frag0, b1a = lds0 + frag1;
    asm volatile("" : "+v"(b0), "+v"(b1a));
#pragma unroll
    for (int m = 0; m < 3; ++m) a[m] = *(LAS u32x4*)(size_t)(b0 + m * 2048);
#define GRP(CUR, M, RD, LD) \
      mfma1(acc[M][0], CUR.r[0], a[M & 3]); SB RD SB \
      mfma1(acc[M][1], CUR.r[1], a[M & 3]); LD \
      mfma1(acc[M][2], CUR.r[2], a[M & 3]); touch(a[(M + 1) & 3]); \
      mfma1(acc[M][3], CUR.r[3], a[M & 3]); SB
#define RDK(M) if constexpr (M < 5) a[(M + 3) & 3] = *(LAS u32x4*)(size_t)(cb + co + (M + 3) * 2048); \
               else a[(M + 3) & 3] = *(LAS u32x4*)(size_t)(nb + no + (M - 5) * 2048);
    auto kstep = [&](WSet& cur, WSet& nx1, WSet& nxt, unsigned soff, unsigned cb, auto coc, unsigned nb, auto noc) {
      constexpr int co = decltype(coc)::value, no = decltype(noc)::value;
#define G(M) GRP(cur, M, RDK(M), if constexpr ((M & 1) == 0) w_ld<M / 2>(nxt, voffW, srdW, soff); if constexpr (M == 7) w_wait<8>(nx1);)
      G(0) G(1) G(2) G(3) G(4) G(5) G(6) G(7)
#undef G
    };
    w_wait<8>(w0);
    unsigned so = wbase + 32768 + 4096;
    const unsigned wmid = MAINS * 32768 + wid * 8192;
    for (int n = 0; n < MAINS; n += 2) {
      kstep(w0, w1s, w3, so, b0, IC(0), b1a, IC(0));
      if (n == MAINS - 2) so = wmid + t_woff(0) - 28672;
      kstep(w1s, w2s, w0, so + 28672, b1a, IC(0), b0, IC(A_STAGE));
      bar_nw();
      kstep(w2s, w3, w1s, so + 32768, b0, IC(A_STAGE), b1a, IC(A_STAGE));
      b0 ^= 2 * A_STAGE;
      if (n == MAINS - 2) so = wmid + t_woff(2) - 32768 - 28672;
      kstep(w3, w0, w2s, so + 32768 + 28672, b1a, IC(A_STAGE), b0, IC(0));
      b1a ^= 2 * A_STAGE;
      bar_nw();
      so += 65536;
    }
    u32x4 px[8];
    {
      const i32x4 srdS = make_srd(ws + SLAB_OFF + (size_t)bid * SLAB_BYTES, SLAB_BYTES);
      const i32x4 srdR = make_srd(ws + SLAB_OFF + (size_t)(bid ^ 8) * SLAB_BYTES, SLAB_BYTES);
      WSet ts[NU];
      ts[0] = w0; ts[1] = w1s; ts[2] = w2s;
      auto top = [&](auto uc, auto gc) {
        constexpr int u = decltype(uc)::value, g = decltype(gc)::value, st = t_store(u, g), ld = t_load(u, g), x = t_w0(u) + t_wn(u) * g;
        if constexpr (st >= 0) {
          const f32x4 lo = acc[st >> 1][2 * (st & 1)], hi = acc[st >> 1][2 * (st & 1) + 1];
          x_st<st & 3>(u32x4{pk_bf16(lo[0], lo[1]), pk_bf16(lo[2], lo[3]), pk_bf16(hi[0], hi[1]), pk_bf16(hi[2], hi[3])}, voffW, srdS, wid * 8192 + (st >> 2) * 4096);
        }
        if constexpr (ld >= 0) x_ld<ld & 3>(px[ld], voffW, srdR, wid * 8192 + (ld >> 2) * 4096);
        if constexpr (t_wn(u) > 0) w_ld<x & 3>(ts[x >> 2], voffW, srdW, wmid + t_woff(x >> 2));
        if constexpr (t_wn(u) > 1) w_ld<(x + 1) & 3>(ts[x >> 2], voffW, srdW, wmid + t_woff(x >> 2));
      };
      auto hk = [&](auto uc) {
        constexpr int U = decltype(uc)::value;
        constexpr int co = ((MAINS + U / 4) & 3) * A_STAGE + ((U >> 1) & 1) * 8192;
        constexpr int no = ((MAINS + (U + 1) / 4) & 3) * A_STAGE + (((U + 1) >> 1) & 1) * 8192;
        const unsigned cb = (U & 1) ? frag1 : frag0, nb = (U & 1) ? frag0 : frag1;
        WSet& cur = ts[U];
        w_wait<tail_wait(U)>(cur);
        if constexpr (U == FLAG_U) {
          if (lane == 0) __hip_atomic_store((GAS u64*)(ws + XFLAG_OFF + bid * 256 + wid * 64), tok, RLX_AGENT);
        }
        SB
#define RDR(M) if constexpr (M == 0) a[3] = *(LAS u32x4*)(lds + cb + co + 3 * 2048); \
               else if constexpr (U + 1 < NU) a[M - 1] = *(LAS u32x4*)(lds + nb + no + (M - 1) * 2048);
        GRP(cur, 4, RDR(0), top(uc, IC(0));) GRP(cur, 5, RDR(1), top(uc, IC(1));) GRP(cur, 6, RDR(2), top(uc, IC(2));) GRP(cur, 7, RDR(3), top(uc, IC(3));)
      };
#define HK4(J) hk(IC(4 * J)); hk(IC(4 * J + 1)); hk(IC(4 * J + 2)); hk(IC(4 * J + 3));
      HK4(0) bar_nw(); HK4(1) bar_nw(); HK4(2) bar_nw();
      HK4(3)
      asm volatile("s_nop 7"); SB
    }
    __builtin_amdgcn_s_setprio(2);
    f32x4 ww[4];
#pragma unroll
    for (int r = 0; r < 4; ++r) ww[r] = *(LAS f32x4*)(bw + wid * 64 + g4 * 16 + r * 4);
    f32x4 wt[4];
#pragma unroll
    for (int nn = 0; nn < 4; ++nn) wt[nn] = f32x4{ww[0][nn], ww[1][nn], ww[2][nn], ww[3][nn]};
#pragma unroll
    for (int m = 0; m < 4; ++m) {
      if (m == 0) x_wait<6>(px[0], px[1]); else if (m == 1) x_wait<4>(px[2], px[3]); else if (m == 2) x_wait<2>(px[4], px[5]); else x_wait<0>(px[6], px[7]);
      f32x2 s2 = f32x2{0.f, 0.f};
#pragma unroll
      for (int nn = 0; nn < 4; ++nn)
#pragma unroll
        for (int r = 0; r < 4; r += 2) {
          const unsigned pd = px[2 * m + (nn >> 1)][2 * (nn & 1) + (r >> 1)];
          const f32x2 x = f32x2{acc[4 + m][nn][r], acc[4 + m][nn][r + 1]} + f32x2{__builtin_bit_cast(float, pd << 16), __builtin_bit_cast(float, pd & 0xffff0000u)};
          const f32x2 t = __builtin_elementwise_fma(x * x, f32x2{-0.1029432397f, -0.1029432397f}, f32x2{-2.302208198f, -2.302208198f});
          const f32x2 z = x * t;
          const f32x2 d = f32x2{__builtin_amdgcn_exp2f(z[0]), __builtin_amdgcn_exp2f(z[1])} + f32x2{1.f, 1.f};
          const f32x2 y = x * f32x2{wt[nn][r], wt[nn][r + 1]};
          s2 = __builtin_elementwise_fma(y, f32x2{__builtin_amdgcn_rcpf(d[0]), __builtin_amdgcn_rcpf(d[1])}, s2);
        }
      *(LAS float*)(lds + IDXP_OFF + ((wid * 4 + g4) * 64 + m * 16 + r16) * 4) = s2[0] + s2[1];
    }
  }
  bar();
  if (tid < 64) {
    float v = red[tid] + *(LAS const float*)(lds + BW_OFF + 1024), v2 = 0.f;
#pragma unroll
    for (int i = 0; i < 16; i += 2) { v += *(LAS const float*)(lds + IDXP_OFF + (i * 64 + tid) * 4); v2 += *(LAS const float*)(lds + IDXP_OFF + (i * 64 + 64 + tid) * 4); }
    __hip_atomic_store((GAS unsigned short*)out + sown + tid, (unsigned short)pk_bf16(v + v2, 0.f), RLX_AGENT);
  }
}

extern "C" void kernel_launch(void* const* d_in, const int* in_sizes, int n_in, void* d_out, int out_size, void* d_ws, size_t ws_size,
                              hipStream_t stream) {
  (void)hipFuncSetAttribute((const void*)pnn_kernel, hipFuncAttributeMaxDynamicSharedMemorySize, LDS_BYTES);
  pnn_kernel<<<NWG, 512, LDS_BYTES, stream>>>((const int*)d_in[0], (const char*)d_in[1], (u64)in_sizes[1] * 2ull, (const char*)d_in[2],
                                              (const char*)d_in[3], (const unsigned short*)d_in[4], (const unsigned short*)d_in[5],
                                              (const unsigned short*)d_in[6], (char*)d_ws, (__hip_bfloat16*)d_out);
}
```
